```python
import torch

def _softmax(x, axis=-1):
    return torch.softmax(x, dim=axis)
def _gelu(x):
    return torch.nn.functional.gelu(x, approximate="tanh")
def _silu(x):
    return torch.nn.functional.silu(x)
def _sigmoid(x):
    return torch.sigmoid(x)
def _layernorm(x, axis=-1, eps=1e-5):
    m = x.mean(dim=axis, keepdim=True)
    v = x.var(dim=axis, keepdim=True, unbiased=False)
    return (x - m) / torch.sqrt(v + eps)
def _scatter_add(tgt, idx, src, axis=0):
    return tgt.clone().index_add_(axis, idx, src)
def _segment_sum(src, seg, n):
    out = torch.zeros((n,) + src.shape[1:], dtype=src.dtype)
    return out.index_add_(0, seg, src)
def _pad(x, pw):
    flat = tuple(v for pair in reversed(pw) for v in pair)
    return torch.nn.functional.pad(x, flat)

kernel_name = 'sequential_preLN__gqa_block_cached__mixer_token_mlp__decode'

def setup_inputs():

    _w = (lambda a: (lambda t: t.to(torch.bfloat16) if t.is_floating_point() else t)(torch.as_tensor(a)))
    def _A_setup():
        B, Sq, D = 2, 1, 576
        Hq, Hkv, Sk, Dh = 32, 16, 32768, 128
        assert Hq % Hkv == 0, (Hq, Hkv)
        return {
            "x": _w((lambda *s: torch.randn(*s, dtype=torch.bfloat16) * 0.05)(B, Sq, D)),
            "k_cache": _w((lambda *s: torch.randn(*s, dtype=torch.bfloat16) * 3.2)(B, Hkv, Sk, Dh)),
            "v_cache": _w((lambda *s: torch.randn(*s, dtype=torch.bfloat16) * 0.8)(B, Hkv, Sk, Dh)),
            "wq": _w((lambda *s: torch.randn(*s, dtype=torch.bfloat16) * 3.2)(D, Hq * Dh) * (D ** -0.5)),
            "wo": _w((lambda *s: torch.randn(*s, dtype=torch.bfloat16) * 0.8)(Hq * Dh, D) * ((Hq * Dh) ** -0.5)),
        }
    _A_in = _A_setup()

    def _B_setup():
        B, S, D, F = 2, 1, 576, 4608
        return {
            "x": _w((lambda *s: torch.randn(*s, dtype=torch.bfloat16) * 0.05)(B, S, D)),
            "w1": _w((lambda *s: torch.randn(*s, dtype=torch.bfloat16) * 0.8)(S, F) * (S ** -0.5)),
            "b1": _w((lambda *s: torch.randn(*s, dtype=torch.bfloat16) * 0.8)(F) * 0.02),
            "w2": _w((lambda *s: torch.randn(*s, dtype=torch.bfloat16) * 0.8)(F, S) * (F ** -0.5)),
            "b2": _w((lambda *s: torch.randn(*s, dtype=torch.bfloat16) * 0.8)(S) * 0.02),
        }
    _B_in = _B_setup()

    return {
        "A_x": _A_in["x"],
        "A_k_cache": _A_in["k_cache"],
        "A_v_cache": _A_in["v_cache"],
        "A_wq": _A_in["wq"],
        "A_wo": _A_in["wo"],
        "B_x": _B_in["x"],
        "B_w1": _B_in["w1"],
        "B_b1": _B_in["b1"],
        "B_w2": _B_in["w2"],
        "B_b2": _B_in["b2"],
    }

def reference(A_x, A_k_cache, A_v_cache, A_wq, A_wo, B_x, B_w1, B_b1, B_w2, B_b2):
    def _A_ref(x, k_cache, v_cache, wq, wo):
        B, Sq, D = x.shape
        Hq, Dh = 32, 128
        Hkv = k_cache.shape[1]
        rep = Hq // Hkv
        q = (lambda x, *d: x.permute(*d))((x @ wq).reshape(B, Sq, Hq, Dh), 0, 2, 1, 3)
        k = (lambda x, n, axis: torch.repeat_interleave(x, n, dim=axis))(k_cache, rep, axis=1)
        v = (lambda x, n, axis: torch.repeat_interleave(x, n, dim=axis))(v_cache, rep, axis=1)
        scores = torch.einsum("bhqd,bhkd->bhqk", q, k) * (Dh ** -0.5)
        p = _softmax(scores, axis=-1)
        o = torch.einsum("bhqk,bhkd->bhqd", p, v)
        return (lambda x, *d: x.permute(*d))(o, 0, 2, 1, 3).reshape(B, Sq, Hq * Dh) @ wo

    def _B_ref(x, w1, b1, w2, b2):
        B, S, D = x.shape
        h = _layernorm(x, axis=-1)
        u = torch.einsum("bsd,sf->bdf", h, w1) + b1
        u = _gelu(u)
        y = torch.einsum("bdf,fs->bds", u, w2) + b2
        return (lambda x, *d: x.permute(*d))(y, 0, 2, 1)

    x = A_x
    h = x + _A_ref(_layernorm(x, axis=-1), A_k_cache, A_v_cache, A_wq, A_wo)
    h2 = h + _B_ref(_layernorm(h, axis=-1), B_w1, B_b1, B_w2, B_b2)
    return h2
```

```cpp
#include <hip/hip_runtime.h>
#include <hip/hip_bf16.h>
#include <stdint.h>

#define MIMIC 0

typedef unsigned u32x4 __attribute__((ext_vector_type(4)));
typedef unsigned u32x2 __attribute__((ext_vector_type(2)));
typedef float f32x4 __attribute__((ext_vector_type(4)));
typedef __bf16 bf16x8 __attribute__((ext_vector_type(8)));
typedef unsigned short u16;

constexpr int DM = 576, HQ = 32, HKV = 16, SK = 32768, DH = 128, FF = 4608;
constexpr int NSPLIT = 8, KEYS_WG = SK / NSPLIT;
constexpr float LOG2E = 1.4426950408889634f;
constexpr float SCALE = 0.08838834764831845f;

__device__ __forceinline__ float bf2f(u16 v) { return __uint_as_float((unsigned)v << 16); }
__device__ __forceinline__ unsigned cvtpk(float lo, float hi) {
  typedef float f32x2_ __attribute__((ext_vector_type(2)));
  typedef __bf16 bf16x2_ __attribute__((ext_vector_type(2)));
  f32x2_ v = {lo, hi};
  return __builtin_bit_cast(unsigned, __builtin_convertvector(v, bf16x2_));
}
__device__ __forceinline__ float rbf(float x) {
  return x;
}
__device__ __forceinline__ float rbf_always(float x) { return __uint_as_float(cvtpk(x, x) << 16); }
__device__ __forceinline__ float wave_sum(float v) {
#pragma unroll
  for (int o = 32; o >= 1; o >>= 1) v += __shfl_xor(v, o);
  return v;
}
__device__ __forceinline__ void ln9(float (&v)[9]) {
  float s = 0.f;
#pragma unroll
  for (int i = 0; i < 9; i++) s += v[i];
  const float mean = wave_sum(s) * (1.f / DM);
  float q = 0.f;
#pragma unroll
  for (int i = 0; i < 9; i++) { float d = v[i] - mean; q += d * d; }
  const float var = rbf(wave_sum(q) * (1.f / DM));
  const float mb = rbf(mean);
  const float den = rbf(sqrtf(rbf(var + 1e-5f)));
#pragma unroll
  for (int i = 0; i < 9; i++) v[i] = rbf(rbf(v[i] - mb) / den);
}

__global__ void __launch_bounds__(256) k_q(const u16* x, const u16* wq, u16* q) {
  __shared__ float ln[2][DM];
  const int lane = threadIdx.x & 63, w = threadIdx.x >> 6;
  if (w < 2) {
    float v[9];
    for (int i = 0; i < 9; i++) v[i] = bf2f(x[w * DM + lane + 64 * i]);
    ln9(v);
    for (int i = 0; i < 9; i++) ln[w][lane + 64 * i] = rbf_always(v[i]);
  }
  __syncthreads();
  __shared__ float red[4][2][64];
  const int col = blockIdx.x * 64 + lane;
  float a0 = 0, a1 = 0;
  for (int k = w * 144; k < w * 144 + 144; k++) { float wv = bf2f(wq[(size_t)k * 4096 + col]); a0 += ln[0][k] * wv; a1 += ln[1][k] * wv; }
  red[w][0][lane] = a0; red[w][1][lane] = a1;
  __syncthreads();
  if (w < 2) {
    float s = red[0][w][lane] + red[1][w][lane] + red[2][w][lane] + red[3][w][lane];
    q[w * 4096 + col] = (u16)(cvtpk(s, s) & 0xffff);
  }
}

constexpr int NST = 2;
__global__ void __launch_bounds__(512) k_attn(const u16* q, const char* K, const char* V, float* pm, float* pl, float* po) {
  __shared__ float sm[8][2], sl[8][2];
  __shared__ __attribute__((aligned(16))) float so[8][2][DH];
  const int lane = threadIdx.x & 63;
  const int wave = __builtin_amdgcn_readfirstlane(threadIdx.x >> 6);
  const int bk = blockIdx.x >> 3, sp = blockIdx.x & 7;
  const int b = bk >> 4, kvh = bk & 15;
  const int g = lane >> 4, n = lane & 15;
  const int h = n >> 3;
  bf16x8 qf[4];
  {
    const u16* qp = q + b * 4096 + (kvh * 2 + h) * DH + 8 * g;
#pragma unroll
    for (int s = 0; s < 4; s++) qf[s] = *(const bf16x8*)(qp + 32 * s);
  }
  unsigned msk[4];
#pragma unroll
  for (int k = 0; k < 4; k++) msk[k] = (((n & 7) >> 1) == k) ? ((n & 1) ? 0xffff0000u : 0x0000ffffu) : 0u;
  auto rk = __builtin_amdgcn_make_buffer_rsrc((void*)K, 0, 256u << 20, 0x00020000);
  auto rv = __builtin_amdgcn_make_buffer_rsrc((void*)V, 0, 256u << 20, 0x00020000);
  const unsigned koff = (unsigned)(4 * (n & 3) + (n >> 2)) * 256 + g * 16 + wave * 4096;
  const unsigned voff = lane * 16 + wave * 4096;
  unsigned so_ = (unsigned)blockIdx.x << 20;
  u32x4 kb[NST][4], vb[NST][4];
#pragma unroll
  for (int s = 0; s < NST; s++) {
#pragma unroll
    for (int t = 0; t < 4; t++) kb[s][t] = __builtin_amdgcn_raw_buffer_load_b128(rk, koff + t * 64, so_ + s * 32768, 2);
#pragma unroll
    for (int t = 0; t < 4; t++) vb[s][t] = __builtin_amdgcn_raw_buffer_load_b128(rv, voff + t * 1024, so_ + s * 32768, 2);
    __builtin_amdgcn_sched_barrier(0);
  }
  float m = -INFINITY, m2 = -INFINITY, l = 0.f;
  f32x4 acc = {0, 0, 0, 0};
  constexpr int NT = KEYS_WG / 128;
#pragma unroll 1
  for (int i = 0; i < NT / NST; i++) {
    so_ += NST * 32768;
    const bool more = i + 1 < NT / NST;
#pragma unroll
    for (int s = 0; s < NST; s++) {
      f32x4 sc = {0, 0, 0, 0};
#pragma unroll
      for (int t = 0; t < 4; t++) sc = __builtin_amdgcn_mfma_f32_16x16x32_bf16(__builtin_bit_cast(bf16x8, kb[s][t]), qf[t], sc, 0, 0, 0);
      __builtin_amdgcn_sched_barrier(0);
      if (more) {
#pragma unroll
        for (int t = 0; t < 4; t++) kb[s][t] = __builtin_amdgcn_raw_buffer_load_b128(rk, koff + t * 64, so_ + s * 32768, 2);
      }
      __builtin_amdgcn_sched_barrier(0);
      float z[4];
#pragma unroll
      for (int j = 0; j < 4; j++) z[j] = rbf(rbf(sc[j]) * SCALE);
      float mx = fmaxf(fmaxf(z[0], z[1]), fmaxf(z[2], z[3]));
      if (__builtin_amdgcn_ballot_w64(mx > m) != 0) {
        mx = fmaxf(mx, __shfl_xor(mx, 16));
        mx = fmaxf(mx, __shfl_xor(mx, 32));
        const float mn = fmaxf(m, mx);
        const float mn2 = mn * LOG2E;
        const float al = __builtin_amdgcn_exp2f(m2 - mn2);
        m = mn; m2 = mn2; l *= al;
        const float a0 = __shfl(al, 0), a1 = __shfl(al, 8);
        const float aa = lane < 32 ? a0 : a1;
        acc *= aa;
      }
#pragma unroll
      for (int t = 0; t < 4; t++) {
        const float p = __builtin_amdgcn_exp2f(fmaf(z[t], LOG2E, -m2));
        l += p;
        const unsigned pp = cvtpk(p, p);
        u32x4 a = {pp & msk[0], pp & msk[1], pp & msk[2], pp & msk[3]};
        acc = __builtin_amdgcn_mfma_f32_16x16x32_bf16(__builtin_bit_cast(bf16x8, a), __builtin_bit_cast(bf16x8, vb[s][t]), acc, 0, 0, 0);
      }
      __builtin_amdgcn_sched_barrier(0);
      if (more) {
#pragma unroll
        for (int t = 0; t < 4; t++) vb[s][t] = __builtin_amdgcn_raw_buffer_load_b128(rv, voff + t * 1024, so_ + s * 32768, 2);
      }
      __builtin_amdgcn_sched_barrier(0);
    }
  }
  l += __shfl_xor(l, 16); l += __shfl_xor(l, 32);
  if (lane == 0 || lane == 8) { sm[wave][h] = m2; sl[wave][h] = l; }
  *(f32x4*)&so[wave][g >> 1][8 * n + 4 * (g & 1)] = acc;
  __syncthreads();
  if (threadIdx.x < 256) {
    const int hh = threadIdx.x >> 7, d = threadIdx.x & 127;
    float M = -INFINITY;
#pragma unroll
    for (int w = 0; w < 8; w++) M = fmaxf(M, sm[w][hh]);
    float L = 0, O = 0;
#pragma unroll
    for (int w = 0; w < 8; w++) { float e = __builtin_amdgcn_exp2f(sm[w][hh] - M); L += e * sl[w][hh]; O += e * so[w][hh][d]; }
    const int row = (b * HQ + kvh * 2 + hh) * NSPLIT + sp;
    po[row * DH + d] = O;
    if (d == 0) { pm[row] = M; pl[row] = L; }
  }
}

__global__ void __launch_bounds__(576) k_proj(const float* pm, const float* pl, const float* po, const u16* wo, float* part) {
  __shared__ float o[2][DH];
  const int hq = blockIdx.x, t = threadIdx.x;
  if (t < 256) {
    const int b = t >> 7, d = t & 127;
    const int row = (b * HQ + hq) * NSPLIT;
    float M = -INFINITY;
    for (int s = 0; s < NSPLIT; s++) M = fmaxf(M, pm[row + s]);
    float L = 0, O = 0;
    for (int s = 0; s < NSPLIT; s++) { float e = __builtin_amdgcn_exp2f(pm[row + s] - M); L += e * pl[row + s]; O += e * po[(row + s) * DH + d]; }
    o[b][d] = rbf(O / L);
  }
  __syncthreads();
  float a0 = 0, a1 = 0;
#pragma unroll 8
  for (int d = 0; d < DH; d++) { float w = bf2f(wo[(size_t)(hq * DH + d) * DM + t]); a0 += o[0][d] * w; a1 += o[1][d] * w; }
  part[(hq * 2 + 0) * DM + t] = a0; part[(hq * 2 + 1) * DM + t] = a1;
}

__global__ void __launch_bounds__(256) k_final(const u16* x, const float* part, const u16* w1, const u16* b1, const u16* w2, const u16* b2, u16* out) {
  __shared__ float hs[DM], hn[DM], red[4][4];
  const int b = blockIdx.x / 144, d0 = (blockIdx.x % 144) * 4;
  const int lane = threadIdx.x & 63, w = threadIdx.x >> 6;
  for (int c = threadIdx.x; c < DM; c += 256) {
    float s = 0;
    for (int hq = 0; hq < HQ; hq++) s += part[(hq * 2 + b) * DM + c];
    hs[c] = rbf(bf2f(x[b * DM + c]) + rbf(s));
  }
  __syncthreads();
  if (w == 0) {
    float v[9];
    for (int i = 0; i < 9; i++) v[i] = hs[lane + 64 * i];
    ln9(v); ln9(v);
    for (int i = 0; i < 9; i++) hn[lane + 64 * i] = v[i];
  }
  __syncthreads();
  float a[4] = {0, 0, 0, 0};
  for (int f = threadIdx.x; f < FF; f += 256) {
    const float W1 = bf2f(w1[f]), B1 = bf2f(b1[f]), W2 = bf2f(w2[f]);
#pragma unroll
    for (int e = 0; e < 4; e++) {
      const float u = rbf(rbf(hn[d0 + e] * W1) + B1);
      const float zz = u * fmaf(u * u, -2.f * LOG2E * 0.7978845608028654f * 0.044715f, -2.f * LOG2E * 0.7978845608028654f);
      const float ge = rbf(u * __builtin_amdgcn_rcpf(1.f + __builtin_amdgcn_exp2f(zz)));
      a[e] = fmaf(ge, W2, a[e]);
    }
  }
#pragma unroll
  for (int e = 0; e < 4; e++) { float s = wave_sum(a[e]); if (lane == 0) red[w][e] = s; }
  __syncthreads();
  if (threadIdx.x < 4) {
    const int e = threadIdx.x;
    const float y = rbf(rbf(red[0][e] + red[1][e] + red[2][e] + red[3][e]) + bf2f(b2[0]));
    const float r = hs[d0 + e] + y;
    out[b * DM + d0 + e] = (u16)(cvtpk(r, r) & 0xffff);
  }
}

extern "C" void kernel_launch(void* const* d_in, const int* in_sizes, int n_in, void* d_out, int out_size, void* d_ws, size_t ws_size, hipStream_t stream) {
  const u16* x = (const u16*)d_in[0];
  const char* K = (const char*)d_in[1];
  const char* V = (const char*)d_in[2];
  const u16* wq = (const u16*)d_in[3];
  const u16* wo = (const u16*)d_in[4];
  const u16* w1 = (const u16*)d_in[6];
  const u16* b1 = (const u16*)d_in[7];
  const u16* w2 = (const u16*)d_in[8];
  const u16* b2 = (const u16*)d_in[9];
  char* ws = (char*)d_ws;
  u16* q = (u16*)ws;
  float* pm = (float*)(ws + 16384);
  float* pl = (float*)(ws + 20480);
  float* po = (float*)(ws + 32768);
  float* part = (float*)(ws + 32768 + 262144);
  k_q<<<64, 256, 0, stream>>>(x, wq, q);
  k_attn<<<256, 512, 0, stream>>>(q, K, V, pm, pl, po);
  k_proj<<<32, 576, 0, stream>>>(pm, pl, po, wo, part);
  k_final<<<288, 256, 0, stream>>>(x, part, w1, b1, w2, b2, (u16*)d_out);
}
```

```cpp
#include <hip/hip_runtime.h>
#include <hip/hip_bf16.h>
#include <stdint.h>

#define DIAG 0

typedef unsigned u32x4 __attribute__((ext_vector_type(4)));
typedef unsigned u32x3 __attribute__((ext_vector_type(3)));
typedef unsigned u32x2 __attribute__((ext_vector_type(2)));
typedef float f32x4 __attribute__((ext_vector_type(4)));
typedef float f32x2 __attribute__((ext_vector_type(2)));
typedef __bf16 bf16x8 __attribute__((ext_vector_type(8)));
typedef __bf16 bf16x2 __attribute__((ext_vector_type(2)));
typedef short s16x4 __attribute__((ext_vector_type(4)));
typedef unsigned short u16;
#define LAS __attribute__((address_space(3)))

constexpr int DM = 576, FF = 4608;
constexpr float LOG2E = 1.4426950408889634f;
constexpr float SCALE = 0.08838834764831845f;
constexpr int NST = 4;
constexpr int NTURN = 32 / NST;
constexpr unsigned SPIN_CAP = 1u << 16;
constexpr int AUX_NT = 2, AUX_SC1 = 16;

constexpr unsigned WS_EPOCH = 0;
constexpr unsigned WS_QG = 4096;
constexpr unsigned WS_P1 = WS_QG + 32 * 8 * 2048;
constexpr unsigned WS_ML = WS_P1 + 512 * 1024;
constexpr unsigned WS_P2 = WS_ML + 512 * 16;
constexpr unsigned WS_END = WS_P2 + 16 * DM * 8;
constexpr unsigned WS_STAMP = 2u << 20;

constexpr int L_Q = 0;
constexpr int L_ML = 512;
constexpr int L_O = 640;
constexpr int L_OL = L_O + 8192;
constexpr int L_HS = L_OL + 2 * 1088;
constexpr int L_RED = L_HS + DM * 4;
constexpr int L_WO = L_RED + 256;
constexpr int L_END = L_WO + 32768;

struct Params {
  const u16* x; const char* K; const char* V; const char* wq; const char* wo;
  const u16* w1; const u16* b1; const u16* w2; const u16* b2; u16* out; char* ws;
};

__device__ __forceinline__ float lo2f(unsigned v) { return __uint_as_float(v << 16); }
__device__ __forceinline__ float hi2f(unsigned v) { return __uint_as_float(v & 0xffff0000u); }
__device__ __forceinline__ unsigned cvtpk(float lo, float hi) {
  f32x2 v = {lo, hi};
  return __builtin_bit_cast(unsigned, __builtin_convertvector(v, bf16x2));
}
__device__ __forceinline__ float rbf(float x) { return lo2f(cvtpk(x, x)); }
template <int CTRL> __device__ __forceinline__ float dpp(float x) {
  return __builtin_bit_cast(float, __builtin_amdgcn_mov_dpp(__builtin_bit_cast(int, x), CTRL, 0xf, 0xf, true));
}
constexpr int XOR1 = 0xB1, XOR2 = 0x4E, HMIR = 0x141, MIR = 0x140;
__device__ __forceinline__ float row_sum(float x) {
  x += dpp<XOR1>(x); x += dpp<XOR2>(x); x += dpp<HMIR>(x); x += dpp<MIR>(x); return x;
}
__device__ __forceinline__ float x16_sum(float x) {
  auto s = __builtin_amdgcn_permlane16_swap(__float_as_uint(x), __float_as_uint(x), false, false);
  return __uint_as_float(s[0]) + __uint_as_float(s[1]);
}
__device__ __forceinline__ float x32_sum(float x) {
  auto s = __builtin_amdgcn_permlane32_swap(__float_as_uint(x), __float_as_uint(x), false, false);
  return __uint_as_float(s[0]) + __uint_as_float(s[1]);
}
__device__ __forceinline__ float x16_max(float x) {
  auto s = __builtin_amdgcn_permlane16_swap(__float_as_uint(x), __float_as_uint(x), false, false);
  return fmaxf(__uint_as_float(s[0]), __uint_as_float(s[1]));
}
__device__ __forceinline__ float x32_max(float x) {
  auto s = __builtin_amdgcn_permlane32_swap(__float_as_uint(x), __float_as_uint(x), false, false);
  return fmaxf(__uint_as_float(s[0]), __uint_as_float(s[1]));
}
__device__ __forceinline__ float wave_sum(float x) { return x32_sum(x16_sum(row_sum(x))); }
__device__ __forceinline__ float rdlane(float x, int l) { return __builtin_bit_cast(float, __builtin_amdgcn_readlane(__builtin_bit_cast(int, x), l)); }
__device__ __forceinline__ f32x4 mfma(u32x4 a, u32x4 b, f32x4 c) {
  return __builtin_amdgcn_mfma_f32_16x16x32_bf16(__builtin_bit_cast(bf16x8, a), __builtin_bit_cast(bf16x8, b), c, 0, 0, 0);
}
#define SB() __builtin_amdgcn_sched_barrier(0)
#define PIN(x) do { asm volatile("" : "+v"(x) :: "memory"); __builtin_amdgcn_sched_barrier(0); } while (0)
#define PINM() do { asm volatile("" ::: "memory"); __builtin_amdgcn_sched_barrier(0); } while (0)
#define LDS_BARRIER() do { asm volatile("s_waitcnt lgkmcnt(0)" ::: "memory"); __builtin_amdgcn_s_barrier(); asm volatile("" ::: "memory"); } while (0)
#define STAMP(i) do {} while (0)

template <int N>
__device__ __forceinline__ void gather(__amdgpu_buffer_rsrc_t rs, unsigned voff, unsigned soff, unsigned sstride, unsigned tag, u32x4 (&g)[N]) {
  unsigned pend = (1u << N) - 1;
  for (unsigned spin = 0; spin < SPIN_CAP; spin++) {
#pragma unroll
    for (int s = 0; s < N; s++)
      if ((pend >> s) & 1) g[s] = __builtin_amdgcn_raw_buffer_load_b128(rs, voff, soff + s * sstride, AUX_SC1);
    asm volatile("" ::: "memory");
#pragma unroll
    for (int s = 0; s < N; s++)
      if (__builtin_amdgcn_ballot_w64(g[s][1] != tag || g[s][3] != tag) == 0) pend &= ~(1u << s);
    if (pend == 0) break;
    __builtin_amdgcn_s_sleep(1);
  }
}

__global__ void __launch_bounds__(512, 2) fused_kernel(Params p) {
  __shared__ __attribute__((aligned(16))) char lds[L_END];
  const int tid = threadIdx.x, lane = tid & 63;
  const int wave = __builtin_amdgcn_readfirstlane(tid >> 6);
  const int bid = blockIdx.x;
  const int g = lane >> 4, n = lane & 15;
  STAMP(0);
  auto rws = __builtin_amdgcn_make_buffer_rsrc((void*)p.ws, 0, WS_END, 0x00020000);
  auto rk = __builtin_amdgcn_make_buffer_rsrc((void*)p.K, 0, 256u << 20, 0x00020000);
  auto rv = __builtin_amdgcn_make_buffer_rsrc((void*)p.V, 0, 256u << 20, 0x00020000);
  const unsigned tag = *(const __attribute__((address_space(4))) unsigned*)(p.ws + WS_EPOCH) + 1u;

  unsigned msk[4];
#pragma unroll
  for (int k = 0; k < 4; k++) msk[k] = (((n & 7) >> 1) == k) ? ((n & 1) ? 0xffff0000u : 0x0000ffffu) : 0u;

  const int ab = bid >> 7, kvh = (bid >> 3) & 15, sp = bid & 7;
  const unsigned koff = (unsigned)(4 * (n & 3) + (n >> 2)) * 256 + g * 16 + wave * 4096;
  const unsigned voff = lane * 16 + wave * 4096;
  unsigned so = (unsigned)bid << 20;
  u32x4 kb[NST][4], vb[NST][4];
#define LOADK(s, off) _Pragma("unroll") for (int t = 0; t < 4; t++) kb[s][t] = __builtin_amdgcn_raw_buffer_load_b128(rk, koff + t * 64, (off), AUX_NT)
#define LOADV(s, off) _Pragma("unroll") for (int t = 0; t < 4; t++) vb[s][t] = __builtin_amdgcn_raw_buffer_load_b128(rv, voff + t * 1024, (off), AUX_NT)

  u32x3 xs[3], xm[3];
  u32x4 wb[18];
  if (wave == 0) {
    const int hq = bid >> 3, ks = bid & 7;
    auto rx = __builtin_amdgcn_make_buffer_rsrc((void*)p.x, 0, 2 * DM * 2, 0x00020000);
    auto rq = __builtin_amdgcn_make_buffer_rsrc((void*)p.wq, 0, DM * 4096 * 2, 0x00020000);
#pragma unroll
    for (int i = 0; i < 3; i++) xs[i] = __builtin_amdgcn_raw_buffer_load_b96(rx, lane * 36 + 12 * i, 0, 0);
#pragma unroll
    for (int i = 0; i < 3; i++) xm[i] = __builtin_amdgcn_raw_buffer_load_b96(rx, ((n >> 3) * 32 + 4 * ks + g) * 36 + 12 * i, 0, 0);
#pragma unroll
    for (int t = 0; t < 18; t++) wb[t] = __builtin_amdgcn_raw_buffer_load_b128(rq, g * 18 * 8192 + n * 16, (72 * ks + t) * 8192 + hq * 256, AUX_NT);
  }
  PINM();
  LOADK(0, so); LOADV(0, so);
  PINM();
  if (wave == 0) {
    float s1 = 0.f;
#pragma unroll
    for (int i = 0; i < 9; i++) { const unsigned u = xs[i / 3][i % 3]; s1 += lo2f(u) + hi2f(u); }
    const float mean = x16_sum(row_sum(s1)) * (1.f / DM);
    float s2 = 0.f;
#pragma unroll
    for (int i = 0; i < 9; i++) { const unsigned u = xs[i / 3][i % 3]; const float a = lo2f(u) - mean, c = hi2f(u) - mean; s2 += a * a + c * c; }
    const float var = rbf(x16_sum(row_sum(s2)) * (1.f / DM));
    const float rden = 1.f / rbf(sqrtf(rbf(var + 1e-5f)));
    const float mb = rbf(mean);
    const float mb0 = rdlane(mb, 0), mb1 = rdlane(mb, 32), rd0 = rdlane(rden, 0), rd1 = rdlane(rden, 32);
    const float mb_ = (n < 8) ? mb0 : mb1, rd_ = (n < 8) ? rd0 : rd1;
    f32x4 qa = {0, 0, 0, 0};
#pragma unroll
    for (int t = 0; t < 18; t++) {
      const unsigned u = xm[t / 6][(t % 6) / 2];
      const float xv = (t & 1) ? hi2f(u) : lo2f(u);
      const float y = rbf(xv - mb_) * rd_;
      const unsigned pp = cvtpk(y, y);
      qa = mfma(u32x4{pp & msk[0], pp & msk[1], pp & msk[2], pp & msk[3]}, wb[t], qa);
    }
    __builtin_amdgcn_raw_buffer_store_b128(u32x4{__float_as_uint(qa[0]), tag, __float_as_uint(qa[1]), tag}, rws, lane * 16, WS_QG + bid * 2048, AUX_SC1);
    __builtin_amdgcn_raw_buffer_store_b128(u32x4{__float_as_uint(qa[2]), tag, __float_as_uint(qa[3]), tag}, rws, lane * 16, WS_QG + bid * 2048 + 1024, AUX_SC1);
    STAMP(1);
  }
  if (wave < 2) {
    u32x4 gq[8];
    gather<8>(rws, (lane >> 5) * 1024 + (32 * ab + (lane & 31)) * 16, WS_QG + (2 * kvh + wave) * 8 * 2048, 2048, tag, gq);
    float a = 0.f, c = 0.f;
#pragma unroll
    for (int s = 0; s < 8; s++) { a += __uint_as_float(gq[s][0]); c += __uint_as_float(gq[s][2]); }
    const int d = 8 * n + 4 * (g & 1) + 2 * (lane >> 5);
    *(LAS unsigned*)(LAS char*)(lds + L_Q + wave * 256 + d * 2) = cvtpk(a, c);
    STAMP(2);
  }
#pragma unroll
  for (int s = 1; s < NST; s++) {
    if (s > 1 && wave >= 2) asm volatile("s_waitcnt vmcnt(8)" ::: "memory");
    PINM(); LOADK(s, so + s * 32768); PINM();
    if (wave >= 2) asm volatile("s_waitcnt vmcnt(8)" ::: "memory");
    PINM(); LOADV(s, so + s * 32768); PINM();
  }
  LDS_BARRIER();
  u32x4 qf[4];
#pragma unroll
  for (int s = 0; s < 4; s++) qf[s] = *(LAS u32x4*)(LAS char*)(lds + L_Q + (n >> 3) * 256 + (32 * s + 8 * g) * 2);
  STAMP(3);

  float m = -INFINITY, m2 = -INFINITY, l = 0.f;
  f32x4 acc = {0, 0, 0, 0};
  auto qk = [&](const u32x4 (&k)[4]) {
    f32x4 sc = {0, 0, 0, 0};
#pragma unroll
    for (int t = 0; t < 4; t++) sc = mfma(k[t], qf[t], sc);
    return sc;
  };
  auto pv = [&](f32x4 sc, const u32x4 (&v)[4]) {
    const unsigned u0 = cvtpk(sc[0], sc[1]), u1 = cvtpk(sc[2], sc[3]);
    const unsigned w0 = cvtpk(lo2f(u0) * SCALE, hi2f(u0) * SCALE), w1 = cvtpk(lo2f(u1) * SCALE, hi2f(u1) * SCALE);
    const float z[4] = {lo2f(w0), hi2f(w0), lo2f(w1), hi2f(w1)};
    float mx = fmaxf(fmaxf(z[0], z[1]), fmaxf(z[2], z[3]));
    if (__builtin_amdgcn_ballot_w64(mx > m) != 0) {
      const float mn = fmaxf(m, x32_max(x16_max(mx)));
      const float mn2 = mn * LOG2E;
      const float al = __builtin_amdgcn_exp2f(m2 - mn2);
      m = mn; m2 = mn2; l *= al;
      const float a0 = rdlane(al, 0), a1 = rdlane(al, 8);
      acc *= (lane < 32) ? a0 : a1;
    }
#pragma unroll
    for (int t = 0; t < 4; t++) {
      const float e = __builtin_amdgcn_exp2f(fmaf(z[t], LOG2E, -m2));
      l += e;
      const unsigned pp = cvtpk(e, e);
      acc = mfma(u32x4{pp & msk[0], pp & msk[1], pp & msk[2], pp & msk[3]}, v[t], acc);
    }
  };
#pragma unroll 1
  for (int i = 0; i < NTURN - 1; i++) {
    so += NST * 32768;
    __builtin_amdgcn_s_barrier();
#pragma unroll
    for (int s = 0; s < NST; s++) {
      f32x4 sc = qk(kb[s]);
      PIN(sc); LOADK(s, so + s * 32768); SB();
      pv(sc, vb[s]);
      PIN(acc); LOADV(s, so + s * 32768); SB();
    }
  }
  STAMP(4);
  const int rho = bid & 7, cg = bid >> 3;
  const int fb = bid >> 7, fi = bid & 127;
  const int e0 = (fi * 9) >> 1, e1 = (fi * 9 + 9) >> 1;
  u32x4 wr[4];
  u32x4 w1v, b1v, w2v; unsigned w1t, b1t, w2t, xh, b2v;
  {
    pv(qk(kb[0]), vb[0]);
    PIN(acc);
    {
      auto ro = __builtin_amdgcn_make_buffer_rsrc((void*)p.wo, 0, cg < 18 ? 4096 * DM * 2 : 0, 0x00020000);
#pragma unroll
      for (int t = 0; t < 4; t++) wr[t] = __builtin_amdgcn_raw_buffer_load_b128(ro, (lane >> 2) * 1152 + (lane & 3) * 16, (512 * rho + 64 * wave + 16 * t) * 1152 + 64 * min(cg, 17), AUX_NT);
    }
    w1v = *(const u32x4*)(p.w1 + 8 * tid); b1v = *(const u32x4*)(p.b1 + 8 * tid); w2v = *(const u32x4*)(p.w2 + 8 * tid);
    w1t = p.w1[4096 + tid]; b1t = p.b1[4096 + tid]; w2t = p.w2[4096 + tid];
    xh = *(const unsigned*)(p.x + fb * DM + 2 * min(tid, 287));
    b2v = p.b2[0];
    SB();
#pragma unroll
    for (int s = 1; s < NST; s++) { pv(qk(kb[s]), vb[s]); SB(); }
  }
  STAMP(5);
  l = x32_sum(x16_sum(l));
  if (lane == 0 || lane == 8) {
    *(LAS float*)(LAS char*)(lds + L_ML + (n >> 3) * 32 + wave * 4) = m2;
    *(LAS float*)(LAS char*)(lds + L_ML + 64 + (n >> 3) * 32 + wave * 4) = l;
  }
  *(LAS f32x4*)(LAS char*)(lds + L_O + wave * 1024 + (g >> 1) * 512 + (8 * n + 4 * (g & 1)) * 4) = acc;
  LDS_BARRIER();
  if (wave < 2) {
    const f32x4 ma = *(LAS f32x4*)(LAS char*)(lds + L_ML + wave * 32), mb = *(LAS f32x4*)(LAS char*)(lds + L_ML + wave * 32 + 16);
    const f32x4 la = *(LAS f32x4*)(LAS char*)(lds + L_ML + 64 + wave * 32), lb = *(LAS f32x4*)(LAS char*)(lds + L_ML + 64 + wave * 32 + 16);
    const float M = fmaxf(fmaxf(fmaxf(ma[0], ma[1]), fmaxf(ma[2], ma[3])), fmaxf(fmaxf(mb[0], mb[1]), fmaxf(mb[2], mb[3])));
    float L = 0.f, o0 = 0.f, o1 = 0.f;
#pragma unroll
    for (int w = 0; w < 8; w++) {
      const float e = __builtin_amdgcn_exp2f((w < 4 ? ma[w & 3] : mb[w & 3]) - M);
      const f32x2 o = *(LAS f32x2*)(LAS char*)(lds + L_O + w * 1024 + wave * 512 + lane * 8);
      L = fmaf(e, w < 4 ? la[w & 3] : lb[w & 3], L); o0 = fmaf(e, o[0], o0); o1 = fmaf(e, o[1], o1);
    }
    const unsigned slot = (ab * 32 + 2 * kvh + wave) * 8 + sp;
    __builtin_amdgcn_raw_buffer_store_b128(u32x4{__float_as_uint(o0), tag, __float_as_uint(o1), tag}, rws, lane * 16, WS_P1 + slot * 1024, AUX_SC1);
    if (lane == 0) __builtin_amdgcn_raw_buffer_store_b128(u32x4{__float_as_uint(M), tag, __float_as_uint(L), tag}, rws, 0, WS_ML + slot * 16, AUX_SC1);
  }
  STAMP(6);
  if (cg < 18) {
#pragma unroll
    for (int t = 0; t < 4; t++) {
      const int row = 64 * wave + 16 * t + (lane >> 2);
      *(LAS u32x4*)(LAS char*)(lds + L_WO + row * 64 + (((lane & 3) ^ ((row >> 2) & 2)) << 4)) = wr[t];
    }
    LDS_BARRIER();
    u32x4 bfr[16];
    if (wave < 2) {
#pragma unroll
      for (int s = 0; s < 16; s++) {
        const int r0 = 32 * s + 8 * g + (n >> 2);
        const int c = ((wave ^ (g & 1)) << 5) + (n & 3) * 8;
        const s16x4 a = __builtin_amdgcn_ds_read_tr16_b64_v4i16((LAS s16x4*)(LAS char*)(lds + L_WO + r0 * 64 + c));
        const s16x4 b = __builtin_amdgcn_ds_read_tr16_b64_v4i16((LAS s16x4*)(LAS char*)(lds + L_WO + (r0 + 4) * 64 + c));
        const u32x2 ua = __builtin_bit_cast(u32x2, a), ub = __builtin_bit_cast(u32x2, b);
        bfr[s] = u32x4{ua[0], ua[1], ub[0], ub[1]};
      }
    }
    const unsigned hrow = (wave >> 2) * 32 + 4 * rho + (wave & 3);
    u32x4 gm[1], go[8];
    gather<8>(rws, lane * 16, WS_P1 + hrow * 8192, 1024, tag, go);
    gather<1>(rws, (lane & 7) * 16, WS_ML + hrow * 128, 0, tag, gm);
    STAMP(7);
    const float ms = __uint_as_float(gm[0][0]);
    float M = fmaxf(ms, dpp<XOR1>(ms)); M = fmaxf(M, dpp<XOR2>(M)); M = fmaxf(M, dpp<HMIR>(M));
    const float e = __builtin_amdgcn_exp2f(ms - M);
    float L = e * __uint_as_float(gm[0][2]); L += dpp<XOR1>(L); L += dpp<XOR2>(L); L += dpp<HMIR>(L);
    const float wgt = e / L;
    float o0 = 0.f, o1 = 0.f;
#pragma unroll
    for (int s = 0; s < 8; s++) { const float ws_ = rdlane(wgt, s); o0 = fmaf(ws_, __uint_as_float(go[s][0]), o0); o1 = fmaf(ws_, __uint_as_float(go[s][2]), o1); }
    *(LAS unsigned*)(LAS char*)(lds + L_OL + (wave >> 2) * 1088 + (wave & 3) * 256 + lane * 4) = cvtpk(o0, o1);
    LDS_BARRIER();
    if (wave < 2) {
      f32x4 d = {0, 0, 0, 0};
#pragma unroll
      for (int s = 0; s < 16; s++) d = mfma(*(LAS u32x4*)(LAS char*)(lds + L_OL + (n & 1) * 1088 + (32 * s + 8 * g) * 2), bfr[s], d);
      if (g < 2) __builtin_amdgcn_raw_buffer_store_b64(u32x2{__float_as_uint(g ? d[1] : d[0]), tag}, rws, (g * 8 * DM + n) * 8, WS_P2 + (rho * DM + 32 * cg + 16 * wave) * 8, AUX_SC1);
    }
    STAMP(8);
  }
  if (wave < 5) {
    u32x4 gp[8];
    gather<8>(rws, min(tid, 287) * 16, WS_P2 + fb * 8 * DM * 8, DM * 8, tag, gp);
    float a = 0.f, c = 0.f;
#pragma unroll
    for (int s = 0; s < 8; s++) { a += __uint_as_float(gp[s][0]); c += __uint_as_float(gp[s][2]); }
    const unsigned at = cvtpk(a, c);
    const unsigned hh = cvtpk(lo2f(xh) + lo2f(at), hi2f(xh) + hi2f(at));
    if (tid < 288) *(LAS f32x2*)(LAS char*)(lds + L_HS + tid * 8) = f32x2{lo2f(hh), hi2f(hh)};
  }
  STAMP(9);
  LDS_BARRIER();
  if (bid == 0 && tid == 0) __builtin_amdgcn_raw_buffer_store_b32(tag, rws, 0, WS_EPOCH, AUX_SC1);
  float hv[5], hn[5];
  {
    float v[9];
#pragma unroll
    for (int i = 0; i < 9; i++) v[i] = *(LAS float*)(LAS char*)(lds + L_HS + (lane + 64 * i) * 4);
#pragma unroll
    for (int e = 0; e < 5; e++) hn[e] = hv[e] = *(LAS float*)(LAS char*)(lds + L_HS + min(e0 + e, DM - 1) * 4);
#pragma unroll
    for (int rep = 0; rep < 2; rep++) {
      float s1 = 0.f;
#pragma unroll
      for (int i = 0; i < 9; i++) s1 += v[i];
      const float mean = wave_sum(s1) * (1.f / DM);
      float s2 = 0.f;
#pragma unroll
      for (int i = 0; i < 9; i++) { const float a = v[i] - mean; s2 = fmaf(a, a, s2); }
      const float var = rbf(wave_sum(s2) * (1.f / DM));
      const float rden = 1.f / rbf(sqrtf(rbf(var + 1e-5f)));
      const float mb = rbf(mean);
      if (rep == 0) {
#pragma unroll
        for (int i = 0; i < 9; i++) v[i] = rbf(rbf(v[i] - mb) * rden);
      }
#pragma unroll
      for (int e = 0; e < 5; e++) hn[e] = rbf(rbf(hn[e] - mb) * rden);
    }
  }
  STAMP(10);
  float ya[5] = {0, 0, 0, 0, 0};
  constexpr float C0 = -2.f * LOG2E * 0.7978845608028654f, C1 = C0 * 0.044715f;
#pragma unroll
  for (int k = 0; k < 9; k++) {
    const float W1 = k == 8 ? lo2f(w1t) : (k & 1) ? hi2f(w1v[k >> 1]) : lo2f(w1v[k >> 1]);
    const float B1 = k == 8 ? lo2f(b1t) : (k & 1) ? hi2f(b1v[k >> 1]) : lo2f(b1v[k >> 1]);
    const float W2 = k == 8 ? lo2f(w2t) : (k & 1) ? hi2f(w2v[k >> 1]) : lo2f(w2v[k >> 1]);
#pragma unroll
    for (int e = 0; e < 5; e++) {
      const float u = fmaf(hn[e], W1, B1);
      const float zz = u * fmaf(u * u, C1, C0);
      ya[e] = fmaf(u * W2, __builtin_amdgcn_rcpf(1.f + __builtin_amdgcn_exp2f(zz)), ya[e]);
    }
  }
#pragma unroll
  for (int e = 0; e < 5; e++) { const float s = wave_sum(ya[e]); if (lane == 0) *(LAS float*)(LAS char*)(lds + L_RED + e * 32 + wave * 4) = s; }
  STAMP(11);
  LDS_BARRIER();
  if (tid < e1 - e0) {
    const f32x4 a = *(LAS f32x4*)(LAS char*)(lds + L_RED + tid * 32), c = *(LAS f32x4*)(LAS char*)(lds + L_RED + tid * 32 + 16);
    const float y = rbf(rbf(((a[0] + a[1]) + (a[2] + a[3])) + ((c[0] + c[1]) + (c[2] + c[3]))) + lo2f(b2v));
    const float hsel = tid == 0 ? hv[0] : tid == 1 ? hv[1] : tid == 2 ? hv[2] : tid == 3 ? hv[3] : hv[4];
    p.out[fb * DM + e0 + tid] = (u16)cvtpk(hsel + y, 0.f);
  }
  STAMP(12);
}

extern "C" void kernel_launch(void* const* d_in, const int* in_sizes, int n_in, void* d_out, int out_size, void* d_ws, size_t ws_size, hipStream_t stream) {
  Params p;
  p.x = (const u16*)d_in[0]; p.K = (const char*)d_in[1]; p.V = (const char*)d_in[2];
  p.wq = (const char*)d_in[3]; p.wo = (const char*)d_in[4];
  p.w1 = (const u16*)d_in[6]; p.b1 = (const u16*)d_in[7]; p.w2 = (const u16*)d_in[8]; p.b2 = (const u16*)d_in[9];
  p.out = (u16*)d_out; p.ws = (char*)d_ws;
  fused_kernel<<<256, 512, 0, stream>>>(p);
}
```

```cpp
#include <hip/hip_runtime.h>
#include <hip/hip_bf16.h>
#include <stdint.h>
#include <stdio.h>

#define DIAG 0

typedef unsigned u32x4 __attribute__((ext_vector_type(4)));
typedef unsigned u32x3 __attribute__((ext_vector_type(3)));
typedef unsigned u32x2 __attribute__((ext_vector_type(2)));
typedef float f32x4 __attribute__((ext_vector_type(4)));
typedef float f32x2 __attribute__((ext_vector_type(2)));
typedef __bf16 bf16x8 __attribute__((ext_vector_type(8)));
typedef __bf16 bf16x2 __attribute__((ext_vector_type(2)));
typedef short s16x4 __attribute__((ext_vector_type(4)));
typedef unsigned short u16;
#define LAS __attribute__((address_space(3)))

constexpr int DM = 576, FF = 4608;
constexpr float LOG2E = 1.4426950408889634f;
constexpr float SCALE = 0.08838834764831845f;
constexpr int NST = 4;
constexpr int NTURN = 32 / NST;
constexpr unsigned SPIN_CAP = 1u << 16;
constexpr int AUX_NT = 2, AUX_SC1 = 16;

constexpr unsigned WS_EPOCH = 0;
constexpr unsigned WS_QG = 4096;
constexpr unsigned WS_P1 = WS_QG + 32 * 8 * 2048;
constexpr unsigned WS_ML = WS_P1 + 512 * 1024;
constexpr unsigned WS_P2 = WS_ML + 512 * 16;
constexpr unsigned WS_END = WS_P2 + 16 * DM * 8;
constexpr unsigned WS_STAMP = 2u << 20;

constexpr int L_Q = 0;
constexpr int L_ML = 512;
constexpr int L_O = 640;
constexpr int L_OL = L_O + 8192;
constexpr int L_HS = L_OL + 2 * 1088;
constexpr int L_RED = L_HS + DM * 4;
constexpr int L_WO = L_RED + 256;
constexpr int L_END = L_WO + 32768;

struct Params {
  const u16* x; const char* K; const char* V; const char* wq; const char* wo;
  const u16* w1; const u16* b1; const u16* w2; const u16* b2; u16* out; char* ws;
};

__device__ __forceinline__ float lo2f(unsigned v) { return __uint_as_float(v << 16); }
__device__ __forceinline__ float hi2f(unsigned v) { return __uint_as_float(v & 0xffff0000u); }
__device__ __forceinline__ unsigned cvtpk(float lo, float hi) {
  f32x2 v = {lo, hi};
  return __builtin_bit_cast(unsigned, __builtin_convertvector(v, bf16x2));
}
__device__ __forceinline__ float rbf(float x) { return lo2f(cvtpk(x, x)); }
template <int CTRL> __device__ __forceinline__ float dpp(float x) {
  return __builtin_bit_cast(float, __builtin_amdgcn_mov_dpp(__builtin_bit_cast(int, x), CTRL, 0xf, 0xf, true));
}
constexpr int XOR1 = 0xB1, XOR2 = 0x4E, HMIR = 0x141, MIR = 0x140;
__device__ __forceinline__ float row_sum(float x) {
  x += dpp<XOR1>(x); x += dpp<XOR2>(x); x += dpp<HMIR>(x); x += dpp<MIR>(x); return x;
}
__device__ __forceinline__ float x16_sum(float x) {
  auto s = __builtin_amdgcn_permlane16_swap(__float_as_uint(x), __float_as_uint(x), false, false);
  return __uint_as_float(s[0]) + __uint_as_float(s[1]);
}
__device__ __forceinline__ float x32_sum(float x) {
  auto s = __builtin_amdgcn_permlane32_swap(__float_as_uint(x), __float_as_uint(x), false, false);
  return __uint_as_float(s[0]) + __uint_as_float(s[1]);
}
__device__ __forceinline__ float x16_max(float x) {
  auto s = __builtin_amdgcn_permlane16_swap(__float_as_uint(x), __float_as_uint(x), false, false);
  return fmaxf(__uint_as_float(s[0]), __uint_as_float(s[1]));
}
__device__ __forceinline__ float x32_max(float x) {
  auto s = __builtin_amdgcn_permlane32_swap(__float_as_uint(x), __float_as_uint(x), false, false);
  return fmaxf(__uint_as_float(s[0]), __uint_as_float(s[1]));
}
__device__ __forceinline__ float wave_sum(float x) { return x32_sum(x16_sum(row_sum(x))); }
__device__ __forceinline__ float rdlane(float x, int l) { return __builtin_bit_cast(float, __builtin_amdgcn_readlane(__builtin_bit_cast(int, x), l)); }
__device__ __forceinline__ f32x4 mfma(u32x4 a, u32x4 b, f32x4 c) {
  return __builtin_amdgcn_mfma_f32_16x16x32_bf16(__builtin_bit_cast(bf16x8, a), __builtin_bit_cast(bf16x8, b), c, 0, 0, 0);
}
#define SB() __builtin_amdgcn_sched_barrier(0)
#define PIN(x) do { asm volatile("" : "+v"(x) :: "memory"); __builtin_amdgcn_sched_barrier(0); } while (0)
#define PINM() do { asm volatile("" ::: "memory"); __builtin_amdgcn_sched_barrier(0); } while (0)
#define LDS_BARRIER() do { asm volatile("s_waitcnt lgkmcnt(0)" ::: "memory"); __builtin_amdgcn_s_barrier(); asm volatile("" ::: "memory"); } while (0)
#define STAMP(i) do {} while (0)

template <int N>
__device__ __forceinline__ void gather(__amdgpu_buffer_rsrc_t rs, unsigned voff, unsigned soff, unsigned sstride, unsigned tag, u32x4 (&g)[N]) {
  unsigned pend = (1u << N) - 1;
  for (unsigned spin = 0; spin < SPIN_CAP; spin++) {
#pragma unroll
    for (int s = 0; s < N; s++)
      if ((pend >> s) & 1) g[s] = __builtin_amdgcn_raw_buffer_load_b128(rs, voff, soff + s * sstride, AUX_SC1);
    asm volatile("" ::: "memory");
#pragma unroll
    for (int s = 0; s < N; s++)
      if (__builtin_amdgcn_ballot_w64(g[s][1] != tag || g[s][3] != tag) == 0) pend &= ~(1u << s);
    if (pend == 0) break;
    __builtin_amdgcn_s_sleep(1);
  }
}

__global__ void __launch_bounds__(512, 2) fused_kernel(Params p) {
  __shared__ __attribute__((aligned(16))) char lds[L_END];
  const int tid = threadIdx.x, lane = tid & 63;
  const int wave = __builtin_amdgcn_readfirstlane(tid >> 6);
  const int bid = blockIdx.x;
  const int g = lane >> 4, n = lane & 15;
  STAMP(0);
  auto rws = __builtin_amdgcn_make_buffer_rsrc((void*)p.ws, 0, WS_END, 0x00020000);
  auto rk = __builtin_amdgcn_make_buffer_rsrc((void*)p.K, 0, 256u << 20, 0x00020000);
  auto rv = __builtin_amdgcn_make_buffer_rsrc((void*)p.V, 0, 256u << 20, 0x00020000);
  const unsigned tag = *(const __attribute__((address_space(4))) unsigned*)(p.ws + WS_EPOCH) + 1u;

  unsigned msk[4];
#pragma unroll
  for (int k = 0; k < 4; k++) msk[k] = (((n & 7) >> 1) == k) ? ((n & 1) ? 0xffff0000u : 0x0000ffffu) : 0u;

  const int ab = bid >> 7, kvh = (bid >> 3) & 15, sp = bid & 7;
  const unsigned koff = (unsigned)(4 * (n & 3) + (n >> 2)) * 256 + g * 16 + wave * 4096;
  const unsigned voff = lane * 16 + wave * 4096;
  unsigned so = (unsigned)bid << 20;
  u32x4 kb[NST][4], vb[NST][4];
#define LOADK(s, off) _Pragma("unroll") for (int t = 0; t < 4; t++) kb[s][t] = __builtin_amdgcn_raw_buffer_load_b128(rk, koff + t * 64, (off), AUX_NT)
#define LOADV(s, off) _Pragma("unroll") for (int t = 0; t < 4; t++) vb[s][t] = __builtin_amdgcn_raw_buffer_load_b128(rv, voff + t * 1024, (off), AUX_NT)

  u32x3 xs[3], xm[3];
  u32x4 wb[18];
  if (wave == 0) {
    const int hq = bid >> 3, ks = bid & 7;
    auto rx = __builtin_amdgcn_make_buffer_rsrc((void*)p.x, 0, 2 * DM * 2, 0x00020000);
    auto rq = __builtin_amdgcn_make_buffer_rsrc((void*)p.wq, 0, DM * 4096 * 2, 0x00020000);
#pragma unroll
    for (int i = 0; i < 3; i++) xs[i] = __builtin_amdgcn_raw_buffer_load_b96(rx, lane * 36 + 12 * i, 0, 0);
#pragma unroll
    for (int i = 0; i < 3; i++) xm[i] = __builtin_amdgcn_raw_buffer_load_b96(rx, ((n >> 3) * 32 + 4 * ks + g) * 36 + 12 * i, 0, 0);
#pragma unroll
    for (int t = 0; t < 18; t++) wb[t] = __builtin_amdgcn_raw_buffer_load_b128(rq, g * 18 * 8192 + n * 16, (72 * ks + t) * 8192 + hq * 256, AUX_NT);
  }
  PINM();
  LOADK(0, so); LOADV(0, so);
  PINM();
  if (wave == 0) {
    float s1 = 0.f;
#pragma unroll
    for (int i = 0; i < 9; i++) { const unsigned u = xs[i / 3][i % 3]; s1 += lo2f(u) + hi2f(u); }
    const float mean = x16_sum(row_sum(s1)) * (1.f / DM);
    float s2 = 0.f;
#pragma unroll
    for (int i = 0; i < 9; i++) { const unsigned u = xs[i / 3][i % 3]; const float a = lo2f(u) - mean, c = hi2f(u) - mean; s2 += a * a + c * c; }
    const float var = rbf(x16_sum(row_sum(s2)) * (1.f / DM));
    const float rden = 1.f / rbf(sqrtf(rbf(var + 1e-5f)));
    const float mb = rbf(mean);
    const float mb0 = rdlane(mb, 0), mb1 = rdlane(mb, 32), rd0 = rdlane(rden, 0), rd1 = rdlane(rden, 32);
    const float mb_ = (n < 8) ? mb0 : mb1, rd_ = (n < 8) ? rd0 : rd1;
    f32x4 qa = {0, 0, 0, 0};
#pragma unroll
    for (int t = 0; t < 18; t++) {
      const unsigned u = xm[t / 6][(t % 6) / 2];
      const float xv = (t & 1) ? hi2f(u) : lo2f(u);
      const float y = rbf(xv - mb_) * rd_;
      const unsigned pp = cvtpk(y, y);
      qa = mfma(u32x4{pp & msk[0], pp & msk[1], pp & msk[2], pp & msk[3]}, wb[t], qa);
    }
    __builtin_amdgcn_raw_buffer_store_b128(u32x4{__float_as_uint(qa[0]), tag, __float_as_uint(qa[1]), tag}, rws, lane * 16, WS_QG + bid * 2048, AUX_SC1);
    __builtin_amdgcn_raw_buffer_store_b128(u32x4{__float_as_uint(qa[2]), tag, __float_as_uint(qa[3]), tag}, rws, lane * 16, WS_QG + bid * 2048 + 1024, AUX_SC1);
    STAMP(1);
  }
  if (wave < 2) {
    u32x4 gq[8];
    gather<8>(rws, (lane >> 5) * 1024 + (32 * ab + (lane & 31)) * 16, WS_QG + (2 * kvh + wave) * 8 * 2048, 2048, tag, gq);
    float a = 0.f, c = 0.f;
#pragma unroll
    for (int s = 0; s < 8; s++) { a += __uint_as_float(gq[s][0]); c += __uint_as_float(gq[s][2]); }
    const int d = 8 * n + 4 * (g & 1) + 2 * (lane >> 5);
    *(LAS unsigned*)(LAS char*)(lds + L_Q + wave * 256 + d * 2) = cvtpk(a, c);
    STAMP(2);
  }
#pragma unroll
  for (int s = 1; s < NST; s++) {
    if (s > 1 && wave >= 2) asm volatile("s_waitcnt vmcnt(8)" ::: "memory");
    PINM(); LOADK(s, so + s * 32768); PINM();
    if (wave >= 2) asm volatile("s_waitcnt vmcnt(8)" ::: "memory");
    PINM(); LOADV(s, so + s * 32768); PINM();
  }
  LDS_BARRIER();
  u32x4 qf[4];
#pragma unroll
  for (int s = 0; s < 4; s++) qf[s] = *(LAS u32x4*)(LAS char*)(lds + L_Q + (n >> 3) * 256 + (32 * s + 8 * g) * 2);
  STAMP(3);

  float m = -INFINITY, m2 = -INFINITY, l = 0.f;
  f32x4 acc = {0, 0, 0, 0};
  auto qk = [&](const u32x4 (&k)[4]) {
    f32x4 sc = {0, 0, 0, 0};
#pragma unroll
    for (int t = 0; t < 4; t++) sc = mfma(k[t], qf[t], sc);
    return sc;
  };
  auto pv = [&](f32x4 sc, const u32x4 (&v)[4]) {
    const unsigned u0 = cvtpk(sc[0], sc[1]), u1 = cvtpk(sc[2], sc[3]);
    const unsigned w0 = cvtpk(lo2f(u0) * SCALE, hi2f(u0) * SCALE), w1 = cvtpk(lo2f(u1) * SCALE, hi2f(u1) * SCALE);
    const float z[4] = {lo2f(w0), hi2f(w0), lo2f(w1), hi2f(w1)};
    float mx = fmaxf(fmaxf(z[0], z[1]), fmaxf(z[2], z[3]));
    if (__builtin_amdgcn_ballot_w64(mx > m) != 0) {
      const float mn = fmaxf(m, x32_max(x16_max(mx)));
      const float mn2 = mn * LOG2E;
      const float al = __builtin_amdgcn_exp2f(m2 - mn2);
      m = mn; m2 = mn2; l *= al;
      const float a0 = rdlane(al, 0), a1 = rdlane(al, 8);
      acc *= (lane < 32) ? a0 : a1;
    }
#pragma unroll
    for (int t = 0; t < 4; t++) {
      const float e = __builtin_amdgcn_exp2f(fmaf(z[t], LOG2E, -m2));
      l += e;
      const unsigned pp = cvtpk(e, e);
      acc = mfma(u32x4{pp & msk[0], pp & msk[1], pp & msk[2], pp & msk[3]}, v[t], acc);
    }
  };
#pragma unroll 1
  for (int i = 0; i < NTURN - 1; i++) {
    so += NST * 32768;
    __builtin_amdgcn_s_barrier();
#pragma unroll
    for (int s = 0; s < NST; s++) {
      f32x4 sc = qk(kb[s]);
      PIN(sc); LOADK(s, so + s * 32768); SB();
      pv(sc, vb[s]);
      PIN(acc); LOADV(s, so + s * 32768); SB();
    }
  }
  STAMP(4);
  const int rho = bid & 7, cg = bid >> 3;
  const int fb = bid >> 7, fi = bid & 127;
  const int e0 = (fi * 9) >> 1, e1 = (fi * 9 + 9) >> 1;
  u32x4 wr[4];
  u32x4 w1v, b1v, w2v; unsigned w1t, b1t, w2t, xh, b2v;
  {
    pv(qk(kb[0]), vb[0]);
    PIN(acc);
    {
      auto ro = __builtin_amdgcn_make_buffer_rsrc((void*)p.wo, 0, cg < 18 ? 4096 * DM * 2 : 0, 0x00020000);
#pragma unroll
      for (int t = 0; t < 4; t++) wr[t] = __builtin_amdgcn_raw_buffer_load_b128(ro, (lane >> 2) * 1152 + (lane & 3) * 16, (512 * rho + 64 * wave + 16 * t) * 1152 + 64 * min(cg, 17), AUX_NT);
    }
    w1v = *(const u32x4*)(p.w1 + 8 * tid); b1v = *(const u32x4*)(p.b1 + 8 * tid); w2v = *(const u32x4*)(p.w2 + 8 * tid);
    w1t = p.w1[4096 + tid]; b1t = p.b1[4096 + tid]; w2t = p.w2[4096 + tid];
    xh = *(const unsigned*)(p.x + fb * DM + 2 * min(tid, 287));
    b2v = p.b2[0];
    SB();
#pragma unroll
    for (int s = 1; s < NST; s++) { pv(qk(kb[s]), vb[s]); SB(); }
  }
  STAMP(5);
  l = x32_sum(x16_sum(l));
  if (lane == 0 || lane == 8) {
    *(LAS float*)(LAS char*)(lds + L_ML + (n >> 3) * 32 + wave * 4) = m2;
    *(LAS float*)(LAS char*)(lds + L_ML + 64 + (n >> 3) * 32 + wave * 4) = l;
  }
  *(LAS f32x4*)(LAS char*)(lds + L_O + wave * 1024 + (g >> 1) * 512 + (8 * n + 4 * (g & 1)) * 4) = acc;
  LDS_BARRIER();
  STAMP(13);
  if (wave < 2) {
    const f32x4 ma = *(LAS f32x4*)(LAS char*)(lds + L_ML + wave * 32), mb = *(LAS f32x4*)(LAS char*)(lds + L_ML + wave * 32 + 16);
    const f32x4 la = *(LAS f32x4*)(LAS char*)(lds + L_ML + 64 + wave * 32), lb = *(LAS f32x4*)(LAS char*)(lds + L_ML + 64 + wave * 32 + 16);
    const float M = fmaxf(fmaxf(fmaxf(ma[0], ma[1]), fmaxf(ma[2], ma[3])), fmaxf(fmaxf(mb[0], mb[1]), fmaxf(mb[2], mb[3])));
    float L = 0.f, o0 = 0.f, o1 = 0.f;
#pragma unroll
    for (int w = 0; w < 8; w++) {
      const float e = __builtin_amdgcn_exp2f((w < 4 ? ma[w & 3] : mb[w & 3]) - M);
      const f32x2 o = *(LAS f32x2*)(LAS char*)(lds + L_O + w * 1024 + wave * 512 + lane * 8);
      L = fmaf(e, w < 4 ? la[w & 3] : lb[w & 3], L); o0 = fmaf(e, o[0], o0); o1 = fmaf(e, o[1], o1);
    }
    const unsigned slot = (ab * 32 + 2 * kvh + wave) * 8 + sp;
    __builtin_amdgcn_raw_buffer_store_b128(u32x4{__float_as_uint(o0), tag, __float_as_uint(o1), tag}, rws, lane * 16, WS_P1 + slot * 1024, AUX_SC1);
    if (lane == 0) __builtin_amdgcn_raw_buffer_store_b128(u32x4{__float_as_uint(M), tag, __float_as_uint(L), tag}, rws, 0, WS_ML + slot * 16, AUX_SC1);
  }
  STAMP(6);
  if (cg < 18) {
#pragma unroll
    for (int t = 0; t < 4; t++) {
      const int row = 64 * wave + 16 * t + (lane >> 2);
      *(LAS u32x4*)(LAS char*)(lds + L_WO + row * 64 + (((lane & 3) ^ ((row >> 2) & 2)) << 4)) = wr[t];
    }
    LDS_BARRIER();
    u32x4 bfr[16];
    if (wave < 2) {
#pragma unroll
      for (int s = 0; s < 16; s++) {
        const int r0 = 32 * s + 8 * g + (n >> 2);
        const int c = ((wave ^ (g & 1)) << 5) + (n & 3) * 8;
        const s16x4 a = __builtin_amdgcn_ds_read_tr16_b64_v4i16((LAS s16x4*)(LAS char*)(lds + L_WO + r0 * 64 + c));
        const s16x4 b = __builtin_amdgcn_ds_read_tr16_b64_v4i16((LAS s16x4*)(LAS char*)(lds + L_WO + (r0 + 4) * 64 + c));
        const u32x2 ua = __builtin_bit_cast(u32x2, a), ub = __builtin_bit_cast(u32x2, b);
        bfr[s] = u32x4{ua[0], ua[1], ub[0], ub[1]};
      }
    }
    const unsigned hrow = (wave >> 2) * 32 + 4 * rho + (wave & 3);
    u32x4 gm[1], go[8];
    gather<8>(rws, lane * 16, WS_P1 + hrow * 8192, 1024, tag, go);
    gather<1>(rws, (lane & 7) * 16, WS_ML + hrow * 128, 0, tag, gm);
    STAMP(7);
    const float ms = __uint_as_float(gm[0][0]);
    float M = fmaxf(ms, dpp<XOR1>(ms)); M = fmaxf(M, dpp<XOR2>(M)); M = fmaxf(M, dpp<HMIR>(M));
    const float e = __builtin_amdgcn_exp2f(ms - M);
    float L = e * __uint_as_float(gm[0][2]); L += dpp<XOR1>(L); L += dpp<XOR2>(L); L += dpp<HMIR>(L);
    const float wgt = e / L;
    float o0 = 0.f, o1 = 0.f;
#pragma unroll
    for (int s = 0; s < 8; s++) { const float ws_ = rdlane(wgt, s); o0 = fmaf(ws_, __uint_as_float(go[s][0]), o0); o1 = fmaf(ws_, __uint_as_float(go[s][2]), o1); }
    *(LAS unsigned*)(LAS char*)(lds + L_OL + (wave >> 2) * 1088 + (wave & 3) * 256 + lane * 4) = cvtpk(o0, o1);
    LDS_BARRIER();
    if (wave < 2) {
      f32x4 d = {0, 0, 0, 0};
#pragma unroll
      for (int s = 0; s < 16; s++) d = mfma(*(LAS u32x4*)(LAS char*)(lds + L_OL + (n & 1) * 1088 + (32 * s + 8 * g) * 2), bfr[s], d);
      if (g < 2) __builtin_amdgcn_raw_buffer_store_b64(u32x2{__float_as_uint(g ? d[1] : d[0]), tag}, rws, (g * 8 * DM + n) * 8, WS_P2 + (rho * DM + 32 * cg + 16 * wave) * 8, AUX_SC1);
    }
    STAMP(8);
  }
  if (wave < 5) {
    u32x4 gp[8];
    gather<8>(rws, min(tid, 287) * 16, WS_P2 + fb * 8 * DM * 8, DM * 8, tag, gp);
    float a = 0.f, c = 0.f;
#pragma unroll
    for (int s = 0; s < 8; s++) { a += __uint_as_float(gp[s][0]); c += __uint_as_float(gp[s][2]); }
    const unsigned at = cvtpk(a, c);
    const unsigned hh = cvtpk(lo2f(xh) + lo2f(at), hi2f(xh) + hi2f(at));
    if (tid < 288) *(LAS f32x2*)(LAS char*)(lds + L_HS + tid * 8) = f32x2{lo2f(hh), hi2f(hh)};
  }
  STAMP(9);
  LDS_BARRIER();
  if (bid == 0 && tid == 0) __builtin_amdgcn_raw_buffer_store_b32(tag, rws, 0, WS_EPOCH, AUX_SC1);
  float hv[5], hn[5];
  {
    float v[9];
#pragma unroll
    for (int i = 0; i < 9; i++) v[i] = *(LAS float*)(LAS char*)(lds + L_HS + (lane + 64 * i) * 4);
#pragma unroll
    for (int e = 0; e < 5; e++) hn[e] = hv[e] = *(LAS float*)(LAS char*)(lds + L_HS + min(e0 + e, DM - 1) * 4);
#pragma unroll
    for (int rep = 0; rep < 2; rep++) {
      float s1 = 0.f;
#pragma unroll
      for (int i = 0; i < 9; i++) s1 += v[i];
      const float mean = wave_sum(s1) * (1.f / DM);
      float s2 = 0.f;
#pragma unroll
      for (int i = 0; i < 9; i++) { const float a = v[i] - mean; s2 = fmaf(a, a, s2); }
      const float var = rbf(wave_sum(s2) * (1.f / DM));
      const float rden = 1.f / rbf(sqrtf(rbf(var + 1e-5f)));
      const float mb = rbf(mean);
      if (rep == 0) {
#pragma unroll
        for (int i = 0; i < 9; i++) v[i] = rbf(rbf(v[i] - mb) * rden);
      }
#pragma unroll
      for (int e = 0; e < 5; e++) hn[e] = rbf(rbf(hn[e] - mb) * rden);
    }
  }
  STAMP(10);
  float ya[5] = {0, 0, 0, 0, 0};
  constexpr float C0 = -2.f * LOG2E * 0.7978845608028654f, C1 = C0 * 0.044715f;
#pragma unroll
  for (int k = 0; k < 9; k++) {
    const float W1 = k == 8 ? lo2f(w1t) : (k & 1) ? hi2f(w1v[k >> 1]) : lo2f(w1v[k >> 1]);
    const float B1 = k == 8 ? lo2f(b1t) : (k & 1) ? hi2f(b1v[k >> 1]) : lo2f(b1v[k >> 1]);
    const float W2 = k == 8 ? lo2f(w2t) : (k & 1) ? hi2f(w2v[k >> 1]) : lo2f(w2v[k >> 1]);
#pragma unroll
    for (int e = 0; e < 5; e++) {
      const float u = fmaf(hn[e], W1, B1);
      const float zz = u * fmaf(u * u, C1, C0);
      ya[e] = fmaf(u * W2, __builtin_amdgcn_rcpf(1.f + __builtin_amdgcn_exp2f(zz)), ya[e]);
    }
  }
#pragma unroll
  for (int e = 0; e < 5; e++) { const float s = wave_sum(ya[e]); if (lane == 0) *(LAS float*)(LAS char*)(lds + L_RED + e * 32 + wave * 4) = s; }
  STAMP(11);
  LDS_BARRIER();
  if (tid < e1 - e0) {
    const f32x4 a = *(LAS f32x4*)(LAS char*)(lds + L_RED + tid * 32), c = *(LAS f32x4*)(LAS char*)(lds + L_RED + tid * 32 + 16);
    const float y = rbf(rbf(((a[0] + a[1]) + (a[2] + a[3])) + ((c[0] + c[1]) + (c[2] + c[3]))) + lo2f(b2v));
    const float hsel = tid == 0 ? hv[0] : tid == 1 ? hv[1] : tid == 2 ? hv[2] : tid == 3 ? hv[3] : hv[4];
    p.out[fb * DM + e0 + tid] = (u16)cvtpk(hsel + y, 0.f);
  }
  STAMP(12);
}

extern "C" void kernel_launch(void* const* d_in, const int* in_sizes, int n_in, void* d_out, int out_size, void* d_ws, size_t ws_size, hipStream_t stream) {
  Params p;
  p.x = (const u16*)d_in[0]; p.K = (const char*)d_in[1]; p.V = (const char*)d_in[2];
  p.wq = (const char*)d_in[3]; p.wo = (const char*)d_in[4];
  p.w1 = (const u16*)d_in[6]; p.b1 = (const u16*)d_in[7]; p.w2 = (const u16*)d_in[8]; p.b2 = (const u16*)d_in[9];
  p.out = (u16*)d_out; p.ws = (char*)d_ws;
  for (int i = 0; i < n_in; i++) fprintf(stderr, "in%d %p (%d)\n", i, d_in[i], in_sizes[i]);
  fprintf(stderr, "out %p ws %p %zu\n", d_out, d_ws, ws_size);
  fused_kernel<<<256, 512, 0, stream>>>(p);
}
```

```cpp
#include <hip/hip_runtime.h>
#include <hip/hip_bf16.h>
#include <stdint.h>

#define DIAG 0

typedef unsigned u32x4 __attribute__((ext_vector_type(4)));
typedef unsigned u32x3 __attribute__((ext_vector_type(3)));
typedef unsigned u32x2 __attribute__((ext_vector_type(2)));
typedef float f32x4 __attribute__((ext_vector_type(4)));
typedef float f32x2 __attribute__((ext_vector_type(2)));
typedef __bf16 bf16x8 __attribute__((ext_vector_type(8)));
typedef __bf16 bf16x2 __attribute__((ext_vector_type(2)));
typedef short s16x4 __attribute__((ext_vector_type(4)));
typedef unsigned short u16;
#define LAS __attribute__((address_space(3)))

constexpr int DM = 576, FF = 4608;
constexpr float LOG2E = 1.4426950408889634f;
constexpr float SCALE = 0.08838834764831845f;
constexpr int NST = 4;
constexpr int NTURN = 32 / NST;
constexpr unsigned SPIN_CAP = 1u << 16;
constexpr int AUX_NT = 2, AUX_SC1 = 16;

constexpr unsigned WS_EPOCH = 0;
constexpr unsigned WS_QG = 4096;
constexpr unsigned WS_P1 = WS_QG + 32 * 8 * 2048;
constexpr unsigned WS_ML = WS_P1 + 512 * 1024;
constexpr unsigned WS_P2 = WS_ML + 512 * 16;
constexpr unsigned WS_END = WS_P2 + 16 * DM * 8;
constexpr unsigned WS_STAMP = 2u << 20;

constexpr int L_Q = 0;
constexpr int L_ML = 512;
constexpr int L_O = 640;
constexpr int L_OL = L_O + 8192;
constexpr int L_HS = L_OL + 2 * 1088;
constexpr int L_RED = L_HS + DM * 4;
constexpr int L_WO = L_RED + 256;
constexpr int L_END = L_WO + 32768;

struct Params {
  const u16* x; const char* K; const char* V; const char* wq; const char* wo;
  const u16* w1; const u16* b1; const u16* w2; const u16* b2; u16* out; char* ws;
};

__device__ __forceinline__ float lo2f(unsigned v) { return __uint_as_float(v << 16); }
__device__ __forceinline__ float hi2f(unsigned v) { return __uint_as_float(v & 0xffff0000u); }
__device__ __forceinline__ unsigned cvtpk(float lo, float hi) {
  f32x2 v = {lo, hi};
  return __builtin_bit_cast(unsigned, __builtin_convertvector(v, bf16x2));
}
__device__ __forceinline__ float rbf(float x) { return lo2f(cvtpk(x, x)); }
template <int CTRL> __device__ __forceinline__ float dpp(float x) {
  return __builtin_bit_cast(float, __builtin_amdgcn_mov_dpp(__builtin_bit_cast(int, x), CTRL, 0xf, 0xf, true));
}
constexpr int XOR1 = 0xB1, XOR2 = 0x4E, HMIR = 0x141, MIR = 0x140;
__device__ __forceinline__ float row_sum(float x) {
  x += dpp<XOR1>(x); x += dpp<XOR2>(x); x += dpp<HMIR>(x); x += dpp<MIR>(x); return x;
}
__device__ __forceinline__ float x16_sum(float x) {
  auto s = __builtin_amdgcn_permlane16_swap(__float_as_uint(x), __float_as_uint(x), false, false);
  return __uint_as_float(s[0]) + __uint_as_float(s[1]);
}
__device__ __forceinline__ float x32_sum(float x) {
  auto s = __builtin_amdgcn_permlane32_swap(__float_as_uint(x), __float_as_uint(x), false, false);
  return __uint_as_float(s[0]) + __uint_as_float(s[1]);
}
__device__ __forceinline__ float x16_max(float x) {
  auto s = __builtin_amdgcn_permlane16_swap(__float_as_uint(x), __float_as_uint(x), false, false);
  return fmaxf(__uint_as_float(s[0]), __uint_as_float(s[1]));
}
__device__ __forceinline__ float x32_max(float x) {
  auto s = __builtin_amdgcn_permlane32_swap(__float_as_uint(x), __float_as_uint(x), false, false);
  return fmaxf(__uint_as_float(s[0]), __uint_as_float(s[1]));
}
__device__ __forceinline__ float wave_sum(float x) { return x32_sum(x16_sum(row_sum(x))); }
__device__ __forceinline__ float rdlane(float x, int l) { return __builtin_bit_cast(float, __builtin_amdgcn_readlane(__builtin_bit_cast(int, x), l)); }
__device__ __forceinline__ f32x4 mfma(u32x4 a, u32x4 b, f32x4 c) {
  return __builtin_amdgcn_mfma_f32_16x16x32_bf16(__builtin_bit_cast(bf16x8, a), __builtin_bit_cast(bf16x8, b), c, 0, 0, 0);
}
#define SB() __builtin_amdgcn_sched_barrier(0)
#define PIN(x) do { asm volatile("" : "+v"(x) :: "memory"); __builtin_amdgcn_sched_barrier(0); } while (0)
#define PINM() do { asm volatile("" ::: "memory"); __builtin_amdgcn_sched_barrier(0); } while (0)
#define LDS_BARRIER() do { asm volatile("s_waitcnt lgkmcnt(0)" ::: "memory"); __builtin_amdgcn_s_barrier(); asm volatile("" ::: "memory"); } while (0)
#define STAMP(i) do { if ((i) == 0) pt0 = ptk = __builtin_amdgcn_s_memrealtime(); else if ((i) == 0) ptk = __builtin_amdgcn_s_memrealtime(); } while (0)

template <int N>
__device__ __forceinline__ void gather(__amdgpu_buffer_rsrc_t rs, unsigned voff, unsigned soff, unsigned sstride, unsigned tag, u32x4 (&g)[N]) {
  unsigned pend = (1u << N) - 1;
  for (unsigned spin = 0; spin < SPIN_CAP; spin++) {
#pragma unroll
    for (int s = 0; s < N; s++)
      if ((pend >> s) & 1) g[s] = __builtin_amdgcn_raw_buffer_load_b128(rs, voff, soff + s * sstride, AUX_SC1);
    asm volatile("" ::: "memory");
#pragma unroll
    for (int s = 0; s < N; s++)
      if (__builtin_amdgcn_ballot_w64(g[s][1] != tag || g[s][3] != tag) == 0) pend &= ~(1u << s);
    if (pend == 0) break;
    __builtin_amdgcn_s_sleep(1);
  }
}

__global__ void __launch_bounds__(512, 2) fused_kernel(Params p) {
  __shared__ __attribute__((aligned(16))) char lds[L_END];
  const int tid = threadIdx.x, lane = tid & 63;
  const int wave = __builtin_amdgcn_readfirstlane(tid >> 6);
  const int bid = blockIdx.x;
  const int g = lane >> 4, n = lane & 15;
  unsigned long long pt0 = 0, ptk = 0;
  STAMP(0);
  auto rws = __builtin_amdgcn_make_buffer_rsrc((void*)p.ws, 0, WS_END, 0x00020000);
  auto rk = __builtin_amdgcn_make_buffer_rsrc((void*)p.K, 0, 256u << 20, 0x00020000);
  auto rv = __builtin_amdgcn_make_buffer_rsrc((void*)p.V, 0, 256u << 20, 0x00020000);
  const unsigned tag = *(const __attribute__((address_space(4))) unsigned*)(p.ws + WS_EPOCH) + 1u;

  unsigned msk[4];
#pragma unroll
  for (int k = 0; k < 4; k++) msk[k] = (((n & 7) >> 1) == k) ? ((n & 1) ? 0xffff0000u : 0x0000ffffu) : 0u;

  const int ab = bid >> 7, kvh = (bid >> 3) & 15, sp = bid & 7;
  const unsigned koff = (unsigned)(4 * (n & 3) + (n >> 2)) * 256 + g * 16 + wave * 4096;
  const unsigned voff = lane * 16 + wave * 4096;
  unsigned so = (unsigned)bid << 20;
  u32x4 kb[NST][4], vb[NST][4];
#define LOADK(s, off) _Pragma("unroll") for (int t = 0; t < 4; t++) kb[s][t] = __builtin_amdgcn_raw_buffer_load_b128(rk, koff + t * 64, (off), AUX_NT)
#define LOADV(s, off) _Pragma("unroll") for (int t = 0; t < 4; t++) vb[s][t] = __builtin_amdgcn_raw_buffer_load_b128(rv, voff + t * 1024, (off), AUX_NT)

  u32x3 xs[3], xm[3];
  u32x4 wb[18];
  if (wave == 0) {
    const int hq = bid >> 3, ks = bid & 7;
    auto rx = __builtin_amdgcn_make_buffer_rsrc((void*)p.x, 0, 2 * DM * 2, 0x00020000);
    auto rq = __builtin_amdgcn_make_buffer_rsrc((void*)p.wq, 0, DM * 4096 * 2, 0x00020000);
#pragma unroll
    for (int i = 0; i < 3; i++) xs[i] = __builtin_amdgcn_raw_buffer_load_b96(rx, lane * 36 + 12 * i, 0, 0);
#pragma unroll
    for (int i = 0; i < 3; i++) xm[i] = __builtin_amdgcn_raw_buffer_load_b96(rx, ((n >> 3) * 32 + 4 * ks + g) * 36 + 12 * i, 0, 0);
#pragma unroll
    for (int t = 0; t < 18; t++) wb[t] = __builtin_amdgcn_raw_buffer_load_b128(rq, g * 18 * 8192 + n * 16, (72 * ks + t) * 8192 + hq * 256, AUX_NT);
  }
  PINM();
  LOADK(0, so); LOADV(0, so);
  PINM();
  if (wave == 0) {
    float s1 = 0.f;
#pragma unroll
    for (int i = 0; i < 9; i++) { const unsigned u = xs[i / 3][i % 3]; s1 += lo2f(u) + hi2f(u); }
    const float mean = x16_sum(row_sum(s1)) * (1.f / DM);
    float s2 = 0.f;
#pragma unroll
    for (int i = 0; i < 9; i++) { const unsigned u = xs[i / 3][i % 3]; const float a = lo2f(u) - mean, c = hi2f(u) - mean; s2 += a * a + c * c; }
    const float var = rbf(x16_sum(row_sum(s2)) * (1.f / DM));
    const float rden = 1.f / rbf(sqrtf(rbf(var + 1e-5f)));
    const float mb = rbf(mean);
    const float mb0 = rdlane(mb, 0), mb1 = rdlane(mb, 32), rd0 = rdlane(rden, 0), rd1 = rdlane(rden, 32);
    const float mb_ = (n < 8) ? mb0 : mb1, rd_ = (n < 8) ? rd0 : rd1;
    f32x4 qa = {0, 0, 0, 0};
#pragma unroll
    for (int t = 0; t < 18; t++) {
      const unsigned u = xm[t / 6][(t % 6) / 2];
      const float xv = (t & 1) ? hi2f(u) : lo2f(u);
      const float y = rbf(xv - mb_) * rd_;
      const unsigned pp = cvtpk(y, y);
      qa = mfma(u32x4{pp & msk[0], pp & msk[1], pp & msk[2], pp & msk[3]}, wb[t], qa);
    }
    __builtin_amdgcn_raw_buffer_store_b128(u32x4{__float_as_uint(qa[0]), tag, __float_as_uint(qa[1]), tag}, rws, lane * 16, WS_QG + bid * 2048, AUX_SC1);
    __builtin_amdgcn_raw_buffer_store_b128(u32x4{__float_as_uint(qa[2]), tag, __float_as_uint(qa[3]), tag}, rws, lane * 16, WS_QG + bid * 2048 + 1024, AUX_SC1);
    STAMP(1);
  }
  if (wave < 2) {
    u32x4 gq[8];
    gather<8>(rws, (lane >> 5) * 1024 + (32 * ab + (lane & 31)) * 16, WS_QG + (2 * kvh + wave) * 8 * 2048, 2048, tag, gq);
    float a = 0.f, c = 0.f;
#pragma unroll
    for (int s = 0; s < 8; s++) { a += __uint_as_float(gq[s][0]); c += __uint_as_float(gq[s][2]); }
    const int d = 8 * n + 4 * (g & 1) + 2 * (lane >> 5);
    *(LAS unsigned*)(LAS char*)(lds + L_Q + wave * 256 + d * 2) = cvtpk(a, c);
    STAMP(2);
  }
#pragma unroll
  for (int s = 1; s < NST; s++) {
    if (s > 1 && wave >= 2) asm volatile("s_waitcnt vmcnt(8)" ::: "memory");
    PINM(); LOADK(s, so + s * 32768); PINM();
    if (wave >= 2) asm volatile("s_waitcnt vmcnt(8)" ::: "memory");
    PINM(); LOADV(s, so + s * 32768); PINM();
  }
  LDS_BARRIER();
  u32x4 qf[4];
#pragma unroll
  for (int s = 0; s < 4; s++) qf[s] = *(LAS u32x4*)(LAS char*)(lds + L_Q + (n >> 3) * 256 + (32 * s + 8 * g) * 2);
  STAMP(3);

  float m = -INFINITY, m2 = -INFINITY, l = 0.f;
  f32x4 acc = {0, 0, 0, 0};
  auto qk = [&](const u32x4 (&k)[4]) {
    f32x4 sc = {0, 0, 0, 0};
#pragma unroll
    for (int t = 0; t < 4; t++) sc = mfma(k[t], qf[t], sc);
    return sc;
  };
  auto pv = [&](f32x4 sc, const u32x4 (&v)[4]) {
    const unsigned u0 = cvtpk(sc[0], sc[1]), u1 = cvtpk(sc[2], sc[3]);
    const unsigned w0 = cvtpk(lo2f(u0) * SCALE, hi2f(u0) * SCALE), w1 = cvtpk(lo2f(u1) * SCALE, hi2f(u1) * SCALE);
    const float z[4] = {lo2f(w0), hi2f(w0), lo2f(w1), hi2f(w1)};
    float mx = fmaxf(fmaxf(z[0], z[1]), fmaxf(z[2], z[3]));
    if (__builtin_amdgcn_ballot_w64(mx > m) != 0) {
      const float mn = fmaxf(m, x32_max(x16_max(mx)));
      const float mn2 = mn * LOG2E;
      const float al = __builtin_amdgcn_exp2f(m2 - mn2);
      m = mn; m2 = mn2; l *= al;
      const float a0 = rdlane(al, 0), a1 = rdlane(al, 8);
      acc *= (lane < 32) ? a0 : a1;
    }
#pragma unroll
    for (int t = 0; t < 4; t++) {
      const float e = __builtin_amdgcn_exp2f(fmaf(z[t], LOG2E, -m2));
      l += e;
      const unsigned pp = cvtpk(e, e);
      acc = mfma(u32x4{pp & msk[0], pp & msk[1], pp & msk[2], pp & msk[3]}, v[t], acc);
    }
  };
#pragma unroll 1
  for (int i = 0; i < NTURN - 1; i++) {
    so += NST * 32768;
    __builtin_amdgcn_s_barrier();
#pragma unroll
    for (int s = 0; s < NST; s++) {
      f32x4 sc = qk(kb[s]);
      PIN(sc); LOADK(s, so + s * 32768); SB();
      pv(sc, vb[s]);
      PIN(acc); LOADV(s, so + s * 32768); SB();
    }
  }
  STAMP(4);
  const int rho = bid & 7, cg = bid >> 3;
  const int fb = bid >> 7, fi = bid & 127;
  const int e0 = (fi * 9) >> 1, e1 = (fi * 9 + 9) >> 1;
  u32x4 wr[4];
  u32x4 w1v, b1v, w2v; unsigned w1t, b1t, w2t, xh, b2v;
  {
    pv(qk(kb[0]), vb[0]);
    PIN(acc);
    {
      auto ro = __builtin_amdgcn_make_buffer_rsrc((void*)p.wo, 0, cg < 18 ? 4096 * DM * 2 : 0, 0x00020000);
#pragma unroll
      for (int t = 0; t < 4; t++) wr[t] = __builtin_amdgcn_raw_buffer_load_b128(ro, (lane >> 2) * 1152 + (lane & 3) * 16, (512 * rho + 64 * wave + 16 * t) * 1152 + 64 * min(cg, 17), AUX_NT);
    }
    w1v = *(const u32x4*)(p.w1 + 8 * tid); b1v = *(const u32x4*)(p.b1 + 8 * tid); w2v = *(const u32x4*)(p.w2 + 8 * tid);
    w1t = p.w1[4096 + tid]; b1t = p.b1[4096 + tid]; w2t = p.w2[4096 + tid];
    xh = *(const unsigned*)(p.x + fb * DM + 2 * min(tid, 287));
    b2v = p.b2[0];
    SB();
#pragma unroll
    for (int s = 1; s < NST; s++) { pv(qk(kb[s]), vb[s]); SB(); }
  }
  STAMP(5);
  l = x32_sum(x16_sum(l));
  if (lane == 0 || lane == 8) {
    *(LAS float*)(LAS char*)(lds + L_ML + (n >> 3) * 32 + wave * 4) = m2;
    *(LAS float*)(LAS char*)(lds + L_ML + 64 + (n >> 3) * 32 + wave * 4) = l;
  }
  *(LAS f32x4*)(LAS char*)(lds + L_O + wave * 1024 + (g >> 1) * 512 + (8 * n + 4 * (g & 1)) * 4) = acc;
  LDS_BARRIER();
  STAMP(13);
  if (wave < 2) {
    const f32x4 ma = *(LAS f32x4*)(LAS char*)(lds + L_ML + wave * 32), mb = *(LAS f32x4*)(LAS char*)(lds + L_ML + wave * 32 + 16);
    const f32x4 la = *(LAS f32x4*)(LAS char*)(lds + L_ML + 64 + wave * 32), lb = *(LAS f32x4*)(LAS char*)(lds + L_ML + 64 + wave * 32 + 16);
    const float M = fmaxf(fmaxf(fmaxf(ma[0], ma[1]), fmaxf(ma[2], ma[3])), fmaxf(fmaxf(mb[0], mb[1]), fmaxf(mb[2], mb[3])));
    float L = 0.f, o0 = 0.f, o1 = 0.f;
#pragma unroll
    for (int w = 0; w < 8; w++) {
      const float e = __builtin_amdgcn_exp2f((w < 4 ? ma[w & 3] : mb[w & 3]) - M);
      const f32x2 o = *(LAS f32x2*)(LAS char*)(lds + L_O + w * 1024 + wave * 512 + lane * 8);
      L = fmaf(e, w < 4 ? la[w & 3] : lb[w & 3], L); o0 = fmaf(e, o[0], o0); o1 = fmaf(e, o[1], o1);
    }
    const unsigned slot = (ab * 32 + 2 * kvh + wave) * 8 + sp;
    __builtin_amdgcn_raw_buffer_store_b128(u32x4{__float_as_uint(o0), tag, __float_as_uint(o1), tag}, rws, lane * 16, WS_P1 + slot * 1024, AUX_SC1);
    if (lane == 0) __builtin_amdgcn_raw_buffer_store_b128(u32x4{__float_as_uint(M), tag, __float_as_uint(L), tag}, rws, 0, WS_ML + slot * 16, AUX_SC1);
  }
  STAMP(6);
  if (cg < 18) {
#pragma unroll
    for (int t = 0; t < 4; t++) {
      const int row = 64 * wave + 16 * t + (lane >> 2);
      *(LAS u32x4*)(LAS char*)(lds + L_WO + row * 64 + (((lane & 3) ^ ((row >> 2) & 2)) << 4)) = wr[t];
    }
    LDS_BARRIER();
    u32x4 bfr[16];
    if (wave < 2) {
#pragma unroll
      for (int s = 0; s < 16; s++) {
        const int r0 = 32 * s + 8 * g + (n >> 2);
        const int c = ((wave ^ (g & 1)) << 5) + (n & 3) * 8;
        const s16x4 a = __builtin_amdgcn_ds_read_tr16_b64_v4i16((LAS s16x4*)(LAS char*)(lds + L_WO + r0 * 64 + c));
        const s16x4 b = __builtin_amdgcn_ds_read_tr16_b64_v4i16((LAS s16x4*)(LAS char*)(lds + L_WO + (r0 + 4) * 64 + c));
        const u32x2 ua = __builtin_bit_cast(u32x2, a), ub = __builtin_bit_cast(u32x2, b);
        bfr[s] = u32x4{ua[0], ua[1], ub[0], ub[1]};
      }
    }
    const unsigned hrow = (wave >> 2) * 32 + 4 * rho + (wave & 3);
    u32x4 gm[1], go[8];
    gather<8>(rws, lane * 16, WS_P1 + hrow * 8192, 1024, tag, go);
    gather<1>(rws, (lane & 7) * 16, WS_ML + hrow * 128, 0, tag, gm);
    STAMP(7);
    const float ms = __uint_as_float(gm[0][0]);
    float M = fmaxf(ms, dpp<XOR1>(ms)); M = fmaxf(M, dpp<XOR2>(M)); M = fmaxf(M, dpp<HMIR>(M));
    const float e = __builtin_amdgcn_exp2f(ms - M);
    float L = e * __uint_as_float(gm[0][2]); L += dpp<XOR1>(L); L += dpp<XOR2>(L); L += dpp<HMIR>(L);
    const float wgt = e / L;
    float o0 = 0.f, o1 = 0.f;
#pragma unroll
    for (int s = 0; s < 8; s++) { const float ws_ = rdlane(wgt, s); o0 = fmaf(ws_, __uint_as_float(go[s][0]), o0); o1 = fmaf(ws_, __uint_as_float(go[s][2]), o1); }
    *(LAS unsigned*)(LAS char*)(lds + L_OL + (wave >> 2) * 1088 + (wave & 3) * 256 + lane * 4) = cvtpk(o0, o1);
    LDS_BARRIER();
    if (wave < 2) {
      f32x4 d = {0, 0, 0, 0};
#pragma unroll
      for (int s = 0; s < 16; s++) d = mfma(*(LAS u32x4*)(LAS char*)(lds + L_OL + (n & 1) * 1088 + (32 * s + 8 * g) * 2), bfr[s], d);
      if (g < 2) __builtin_amdgcn_raw_buffer_store_b64(u32x2{__float_as_uint(g ? d[1] : d[0]), tag}, rws, (g * 8 * DM + n) * 8, WS_P2 + (rho * DM + 32 * cg + 16 * wave) * 8, AUX_SC1);
    }
    STAMP(8);
  }
  if (wave < 5) {
    u32x4 gp[8];
    gather<8>(rws, min(tid, 287) * 16, WS_P2 + fb * 8 * DM * 8, DM * 8, tag, gp);
    float a = 0.f, c = 0.f;
#pragma unroll
    for (int s = 0; s < 8; s++) { a += __uint_as_float(gp[s][0]); c += __uint_as_float(gp[s][2]); }
    const unsigned at = cvtpk(a, c);
    const unsigned hh = cvtpk(lo2f(xh) + lo2f(at), hi2f(xh) + hi2f(at));
    if (tid < 288) *(LAS f32x2*)(LAS char*)(lds + L_HS + tid * 8) = f32x2{lo2f(hh), hi2f(hh)};
  }
  STAMP(9);
  LDS_BARRIER();
  if (bid == 0 && tid == 0) __builtin_amdgcn_raw_buffer_store_b32(tag, rws, 0, WS_EPOCH, AUX_SC1);
  float hv[5], hn[5];
  {
    float v[9];
#pragma unroll
    for (int i = 0; i < 9; i++) v[i] = *(LAS float*)(LAS char*)(lds + L_HS + (lane + 64 * i) * 4);
#pragma unroll
    for (int e = 0; e < 5; e++) hn[e] = hv[e] = *(LAS float*)(LAS char*)(lds + L_HS + min(e0 + e, DM - 1) * 4);
#pragma unroll
    for (int rep = 0; rep < 2; rep++) {
      float s1 = 0.f;
#pragma unroll
      for (int i = 0; i < 9; i++) s1 += v[i];
      const float mean = wave_sum(s1) * (1.f / DM);
      float s2 = 0.f;
#pragma unroll
      for (int i = 0; i < 9; i++) { const float a = v[i] - mean; s2 = fmaf(a, a, s2); }
      const float var = rbf(wave_sum(s2) * (1.f / DM));
      const float rden = 1.f / rbf(sqrtf(rbf(var + 1e-5f)));
      const float mb = rbf(mean);
      if (rep == 0) {
#pragma unroll
        for (int i = 0; i < 9; i++) v[i] = rbf(rbf(v[i] - mb) * rden);
      }
#pragma unroll
      for (int e = 0; e < 5; e++) hn[e] = rbf(rbf(hn[e] - mb) * rden);
    }
  }
  STAMP(10);
  float ya[5] = {0, 0, 0, 0, 0};
  constexpr float C0 = -2.f * LOG2E * 0.7978845608028654f, C1 = C0 * 0.044715f;
#pragma unroll
  for (int k = 0; k < 9; k++) {
    const float W1 = k == 8 ? lo2f(w1t) : (k & 1) ? hi2f(w1v[k >> 1]) : lo2f(w1v[k >> 1]);
    const float B1 = k == 8 ? lo2f(b1t) : (k & 1) ? hi2f(b1v[k >> 1]) : lo2f(b1v[k >> 1]);
    const float W2 = k == 8 ? lo2f(w2t) : (k & 1) ? hi2f(w2v[k >> 1]) : lo2f(w2v[k >> 1]);
#pragma unroll
    for (int e = 0; e < 5; e++) {
      const float u = fmaf(hn[e], W1, B1);
      const float zz = u * fmaf(u * u, C1, C0);
      ya[e] = fmaf(u * W2, __builtin_amdgcn_rcpf(1.f + __builtin_amdgcn_exp2f(zz)), ya[e]);
    }
  }
#pragma unroll
  for (int e = 0; e < 5; e++) { const float s = wave_sum(ya[e]); if (lane == 0) *(LAS float*)(LAS char*)(lds + L_RED + e * 32 + wave * 4) = s; }
  STAMP(11);
  LDS_BARRIER();
  if (tid < e1 - e0) {
    const f32x4 a = *(LAS f32x4*)(LAS char*)(lds + L_RED + tid * 32), c = *(LAS f32x4*)(LAS char*)(lds + L_RED + tid * 32 + 16);
    const float y = rbf(rbf(((a[0] + a[1]) + (a[2] + a[3])) + ((c[0] + c[1]) + (c[2] + c[3]))) + lo2f(b2v));
    const float hsel = tid == 0 ? hv[0] : tid == 1 ? hv[1] : tid == 2 ? hv[2] : tid == 3 ? hv[3] : hv[4];
    p.out[fb * DM + e0 + tid] = (u16)cvtpk(hsel + y, 0.f);
  }
  STAMP(12);
  if (wave == 0) { const unsigned long long tend = pt0 + 15000 + 2 * (ptk - pt0); for (int it = 0; it < 2000000 && __builtin_amdgcn_s_memrealtime() < tend; it++) __builtin_amdgcn_s_sleep(1); }
}

extern "C" void kernel_launch(void* const* d_in, const int* in_sizes, int n_in, void* d_out, int out_size, void* d_ws, size_t ws_size, hipStream_t stream) {
  Params p;
  p.x = (const u16*)d_in[0]; p.K = (const char*)d_in[1]; p.V = (const char*)d_in[2];
  p.wq = (const char*)d_in[3]; p.wo = (const char*)d_in[4];
  p.w1 = (const u16*)d_in[6]; p.b1 = (const u16*)d_in[7]; p.w2 = (const u16*)d_in[8]; p.b2 = (const u16*)d_in[9];
  p.out = (u16*)d_out; p.ws = (char*)d_ws;
  fused_kernel<<<256, 512, 0, stream>>>(p);
}
```

```cpp
#include <hip/hip_runtime.h>
#include <hip/hip_bf16.h>
#include <stdint.h>

#define DIAG 0
#define TOUCH0 1
#define TOUCH1 1
#define EARLY 0
#define WQ_AUX 2

typedef unsigned u32x4 __attribute__((ext_vector_type(4)));
typedef unsigned u32x3 __attribute__((ext_vector_type(3)));
typedef unsigned u32x2 __attribute__((ext_vector_type(2)));
typedef float f32x4 __attribute__((ext_vector_type(4)));
typedef float f32x2 __attribute__((ext_vector_type(2)));
typedef __bf16 bf16x8 __attribute__((ext_vector_type(8)));
typedef __bf16 bf16x2 __attribute__((ext_vector_type(2)));
typedef short s16x4 __attribute__((ext_vector_type(4)));
typedef unsigned short u16;
#define LAS __attribute__((address_space(3)))

constexpr int DM = 576, FF = 4608;
constexpr float LOG2E = 1.4426950408889634f;
constexpr float SCALE = 0.08838834764831845f;
constexpr int NST = 4;
constexpr int NTURN = 32 / NST;
constexpr unsigned SPIN_CAP = 1u << 16;
constexpr int AUX_NT = 2, AUX_SC1 = 16;

constexpr unsigned WS_EPOCH = 0;
constexpr unsigned WS_QG = 4096;
constexpr unsigned WS_P1 = WS_QG + 2 * 16384;
constexpr unsigned WS_ML = WS_P1 + 512 * 1024;
constexpr unsigned WS_P2 = WS_ML + 512 * 16;
constexpr unsigned WS_END = WS_P2 + 16 * DM * 8;
constexpr unsigned WS_STAMP = 2u << 20;

constexpr int L_Q = 0;
constexpr int L_ML = 512;
constexpr int L_O = 640;
constexpr int L_OL = L_O + 8192;
constexpr int L_HS = L_OL + 2 * 1088;
constexpr int L_RED = L_HS + DM * 4;
constexpr int L_WO = L_RED + 256;
constexpr int L_END = L_WO + 32768;

struct Params {
  const u16* x; const char* K; const char* V; const char* wq; const char* wo;
  const u16* w1; const u16* b1; const u16* w2; const u16* b2; u16* out; char* ws;
};

__device__ __forceinline__ float lo2f(unsigned v) { return __uint_as_float(v << 16); }
__device__ __forceinline__ float hi2f(unsigned v) { return __uint_as_float(v & 0xffff0000u); }
__device__ __forceinline__ unsigned cvtpk(float lo, float hi) {
  f32x2 v = {lo, hi};
  return __builtin_bit_cast(unsigned, __builtin_convertvector(v, bf16x2));
}
__device__ __forceinline__ float rbf(float x) { return lo2f(cvtpk(x, x)); }
template <int CTRL> __device__ __forceinline__ float dpp(float x) {
  return __builtin_bit_cast(float, __builtin_amdgcn_mov_dpp(__builtin_bit_cast(int, x), CTRL, 0xf, 0xf, true));
}
constexpr int XOR1 = 0xB1, XOR2 = 0x4E, HMIR = 0x141, MIR = 0x140;
__device__ __forceinline__ float row_sum(float x) {
  x += dpp<XOR1>(x); x += dpp<XOR2>(x); x += dpp<HMIR>(x); x += dpp<MIR>(x); return x;
}
__device__ __forceinline__ float x16_add(float a, float b) {
  auto s = __builtin_amdgcn_permlane16_swap(__float_as_uint(a), __float_as_uint(b), false, false);
  return __uint_as_float(s[0]) + __uint_as_float(s[1]);
}
__device__ __forceinline__ float x32_add(float a, float b) {
  auto s = __builtin_amdgcn_permlane32_swap(__float_as_uint(a), __float_as_uint(b), false, false);
  return __uint_as_float(s[0]) + __uint_as_float(s[1]);
}
__device__ __forceinline__ float x16_max(float x) {
  auto s = __builtin_amdgcn_permlane16_swap(__float_as_uint(x), __float_as_uint(x), false, false);
  return fmaxf(__uint_as_float(s[0]), __uint_as_float(s[1]));
}
__device__ __forceinline__ float x32_max(float x) {
  auto s = __builtin_amdgcn_permlane32_swap(__float_as_uint(x), __float_as_uint(x), false, false);
  return fmaxf(__uint_as_float(s[0]), __uint_as_float(s[1]));
}
__device__ __forceinline__ float wave_sum(float x) { x = row_sum(x); x = x16_add(x, x); return x32_add(x, x); }
__device__ __forceinline__ float rdlane(float x, int l) { return __builtin_bit_cast(float, __builtin_amdgcn_readlane(__builtin_bit_cast(int, x), l)); }
__device__ __forceinline__ float rden_of(float var) {
  return __builtin_amdgcn_rcpf(rbf(__builtin_amdgcn_sqrtf(rbf(rbf(var) + 1e-5f))));
}
__device__ __forceinline__ f32x4 mfma(u32x4 a, u32x4 b, f32x4 c) {
  return __builtin_amdgcn_mfma_f32_16x16x32_bf16(__builtin_bit_cast(bf16x8, a), __builtin_bit_cast(bf16x8, b), c, 0, 0, 0);
}
__device__ __forceinline__ u32x4 read_tr(LAS char* a, LAS char* b) {
  const u32x2 ua = __builtin_bit_cast(u32x2, __builtin_amdgcn_ds_read_tr16_b64_v4i16((LAS s16x4*)a));
  const u32x2 ub = __builtin_bit_cast(u32x2, __builtin_amdgcn_ds_read_tr16_b64_v4i16((LAS s16x4*)b));
  return u32x4{ua[0], ua[1], ub[0], ub[1]};
}
#define SB() __builtin_amdgcn_sched_barrier(0)
#define PIN(x) do { asm volatile("" : "+v"(x) :: "memory"); __builtin_amdgcn_sched_barrier(0); } while (0)
#define PINM() do { asm volatile("" ::: "memory"); __builtin_amdgcn_sched_barrier(0); } while (0)
#define SINK(x) asm volatile("" :: "v"(x))
#define VMWAIT(n) do { __builtin_amdgcn_sched_barrier(0); asm volatile("s_waitcnt vmcnt(" #n ")" ::: "memory"); __builtin_amdgcn_sched_barrier(0); } while (0)
#define LDS_BARRIER() do { asm volatile("s_waitcnt lgkmcnt(0)" ::: "memory"); __builtin_amdgcn_s_barrier(); asm volatile("" ::: "memory"); } while (0)
#define STAMP(i) do {} while (0)

__device__ __forceinline__ bool fresh(u32x4 v, unsigned tag) { return __builtin_amdgcn_ballot_w64(v[1] != tag || v[3] != tag) == 0; }
template <int N, class F>
__device__ __forceinline__ void settle(u32x4 (&g)[N], unsigned tag, F load) {
  unsigned pend = (1u << N) - 1;
#pragma unroll 1
  for (unsigned spin = 0; spin < SPIN_CAP; spin++) {
#pragma unroll
    for (int s = 0; s < N; s++)
      if (fresh(g[s], tag)) pend &= ~(1u << s);
    if (pend == 0) break;
    __builtin_amdgcn_s_sleep(1);
    asm volatile("" ::: "memory");
#pragma unroll
    for (int s = 0; s < N; s++)
      if ((pend >> s) & 1) g[s] = load(s);
  }
}

__global__ void __launch_bounds__(512, 2) fused_kernel(Params p) {
  __shared__ __attribute__((aligned(16))) char lds_[L_END];
  LAS char* const lds = (LAS char*)lds_;
  const int tid = threadIdx.x, lane = tid & 63;
  const int wave = __builtin_amdgcn_readfirstlane(tid >> 6);
  const int bid = blockIdx.x;
  const int g = lane >> 4, n = lane & 15;
  STAMP(0);
  auto rws = __builtin_amdgcn_make_buffer_rsrc((void*)p.ws, 0, WS_END, 0x00020000);
  auto rk = __builtin_amdgcn_make_buffer_rsrc((void*)p.K, 0, 256u << 20, 0x00020000);
  auto rv = __builtin_amdgcn_make_buffer_rsrc((void*)p.V, 0, 256u << 20, 0x00020000);
  char* pc; unsigned pclen;
  asm volatile("s_getpc_b64 %0\n.Lfused_first:\n\ts_mov_b32 %1, .Lfused_last-.Lfused_first" : "=s"(pc), "=s"(pclen));
  auto rc = __builtin_amdgcn_make_buffer_rsrc((void*)pc, 0, pclen, 0x00020000);
  const unsigned tag = *(const __attribute__((address_space(4))) unsigned*)(p.ws + WS_EPOCH) + 1u;

  const int ab = bid >> 7, kvh = (bid >> 3) & 15, sp = bid & 7;
  const unsigned koff = (unsigned)(4 * (n & 3) + (n >> 2)) * 256 + g * 16 + wave * 4096;
  const unsigned voff = lane * 16 + wave * 4096;
  unsigned so = (unsigned)bid << 20;
  u32x4 kb[NST][4], vb[NST][4];
#define LOADK(s, off) _Pragma("unroll") for (int t = 0; t < 4; t++) kb[s][t] = __builtin_amdgcn_raw_buffer_load_b128(rk, koff + t * 64, (off), AUX_NT)
#define LOADV(s, off) _Pragma("unroll") for (int t = 0; t < 4; t++) vb[s][t] = __builtin_amdgcn_raw_buffer_load_b128(rv, voff + t * 1024, (off), AUX_NT)

  const int qc = (bid & 7) * 32 + (bid >> 3);
  u32x3 xs[3];
  u32x4 wb[18];
  unsigned touch = 0;
  if (wave == 0) {
    auto rx = __builtin_amdgcn_make_buffer_rsrc((void*)p.x, 0, 2 * DM * 2, 0x00020000);
    auto rq = __builtin_amdgcn_make_buffer_rsrc((void*)p.wq, 0, DM * 4096 * 2, 0x00020000);
#pragma unroll
    for (int i = 0; i < 3; i++) xs[i] = __builtin_amdgcn_raw_buffer_load_b96(rx, lane * 36 + 12 * i, 0, 0);
#pragma unroll
    for (int t = 0; t < 18; t++) wb[t] = __builtin_amdgcn_raw_buffer_load_b128(rq, (lane >> 1) * 8192 + (lane & 1) * 16, t * 32 * 8192 + qc * 32, WQ_AUX);
  }
  PINM();
  touch = __builtin_amdgcn_raw_buffer_load_b32(rc, lane * 64, ((wave + 7) & 7) * 4096, 0);
  LOADK(0, so); LOADV(0, so);
  PINM();
  if (wave != 0) VMWAIT(4);
  LOADK(1, so + 32768); LOADV(1, so + 32768);
  PINM();
  if (wave == 0) {
    float s1 = 0.f;
#pragma unroll
    for (int i = 0; i < 9; i++) { const unsigned u = xs[i / 3][i % 3]; s1 += lo2f(u) + hi2f(u); }
    s1 = row_sum(s1);
    const float mean = x16_add(s1, s1) * (1.f / DM);
    float s2 = 0.f;
#pragma unroll
    for (int i = 0; i < 9; i++) { const unsigned u = xs[i / 3][i % 3]; const float a = lo2f(u) - mean, c = hi2f(u) - mean; s2 += a * a + c * c; }
    s2 = row_sum(s2);
    const float rden = rden_of(x16_add(s2, s2) * (1.f / DM)), mb = rbf(mean);
#pragma unroll
    for (int i = 0; i < 9; i++) {
      const unsigned u = xs[i / 3][i % 3], d = cvtpk(lo2f(u) - mb, hi2f(u) - mb);
      *(LAS unsigned*)(lds + L_O + lane * 36 + 4 * i) = cvtpk(lo2f(d) * rden, hi2f(d) * rden);
    }
    const int r = lane >> 1;
    const int wst = L_WO + 512 * ((r >> 2) & 1) + 128 * (r >> 3) + 32 * (r & 3) + 16 * (lane & 1);
    const int wld = L_WO + 128 * g + 32 * (n >> 2) + 8 * (n & 3);
    f32x4 qa = {0, 0, 0, 0};
#pragma unroll
    for (int c = 0; c < 3; c++) {
#pragma unroll
      for (int t = 6 * c; t < 6 * c + 6; t++) *(LAS u32x4*)(lds + wst + 1024 * t) = wb[t];
      asm volatile("" ::: "memory");
#pragma unroll
      for (int t = 6 * c; t < 6 * c + 6; t++)
        qa = mfma(*(LAS u32x4*)(lds + L_O + (n & 1) * (DM * 2) + 64 * t + 16 * g), read_tr(lds + wld + 1024 * t, lds + wld + 1024 * t + 512), qa);
    }
    const float o0 = dpp<XOR1>(qa[0]), o1 = dpp<XOR1>(qa[1]);
    const unsigned qq = (n & 1) ? cvtpk(o1, qa[1]) : cvtpk(qa[0], o0);
    if (g == 0) __builtin_amdgcn_raw_buffer_store_b64(u32x2{qq, tag}, rws, (n & 1) * 16384 + (n >> 1) * 8, WS_QG + qc * 64, AUX_SC1);
    STAMP(1);
  }
  auto rpoll = __builtin_amdgcn_make_buffer_rsrc((void*)p.ws, 0, wave == 1 ? WS_END : 0, 0x00020000);
  const unsigned qso = WS_QG + ab * 16384 + kvh * 1024;
  u32x4 pq[3];
  VMWAIT(8); SINK(touch);
  LOADK(2, so + 2 * 32768);
  VMWAIT(8);
  pq[0] = __builtin_amdgcn_raw_buffer_load_b128(rpoll, lane * 16, qso, AUX_SC1); PINM();
  LOADV(2, so + 2 * 32768);
  VMWAIT(9);
  pq[1] = __builtin_amdgcn_raw_buffer_load_b128(rpoll, lane * 16, qso, AUX_SC1); PINM();
  LOADK(3, so + 3 * 32768);
  VMWAIT(10);
  pq[2] = __builtin_amdgcn_raw_buffer_load_b128(rpoll, lane * 16, qso, AUX_SC1); PINM();
  LOADV(3, so + 3 * 32768);
  PINM();
  if (wave == 1) {
    u32x2 qv;
    if (fresh(pq[0], tag)) { qv = u32x2{pq[0][0], pq[0][2]}; PIN(qv); }
    else if (fresh(pq[1], tag)) { qv = u32x2{pq[1][0], pq[1][2]}; PIN(qv); }
    else if (fresh(pq[2], tag)) { qv = u32x2{pq[2][0], pq[2][2]}; PIN(qv); }
    else {
      u32x4 q1[1] = {u32x4{0, ~tag, 0, ~tag}};
      settle<1>(q1, tag, [&](int) { return __builtin_amdgcn_raw_buffer_load_b128(rws, lane * 16, qso, AUX_SC1); });
      qv = u32x2{q1[0][0], q1[0][2]}; PIN(qv);
    }
    *(LAS u32x2*)(lds + L_Q + lane * 8) = qv;
    STAMP(2);
  }
  LDS_BARRIER();
  u32x4 qf[4];
#pragma unroll
  for (int s = 0; s < 4; s++) qf[s] = *(LAS u32x4*)(lds + L_Q + (n >> 3) * 256 + (32 * s + 8 * g) * 2);
  unsigned msk[4];
#pragma unroll
  for (int k = 0; k < 4; k++) msk[k] = (((n & 7) >> 1) == k) ? ((n & 1) ? 0xffff0000u : 0x0000ffffu) : 0u;
  STAMP(3);

  float m = -INFINITY, m2 = -INFINITY, l = 0.f;
  f32x4 acc = {0, 0, 0, 0};
  auto qk = [&](const u32x4 (&k)[4]) {
    f32x4 sc = {0, 0, 0, 0};
#pragma unroll
    for (int t = 0; t < 4; t++) sc = mfma(k[t], qf[t], sc);
    return sc;
  };
  auto pv = [&](f32x4 sc, const u32x4 (&v)[4]) {
    const unsigned u0 = cvtpk(sc[0], sc[1]), u1 = cvtpk(sc[2], sc[3]);
    const unsigned w0 = cvtpk(lo2f(u0) * SCALE, hi2f(u0) * SCALE), w1 = cvtpk(lo2f(u1) * SCALE, hi2f(u1) * SCALE);
    const float z[4] = {lo2f(w0), hi2f(w0), lo2f(w1), hi2f(w1)};
    float mx = fmaxf(fmaxf(z[0], z[1]), fmaxf(z[2], z[3]));
    if (__builtin_amdgcn_ballot_w64(mx > m) != 0) {
      const float mn = fmaxf(m, x32_max(x16_max(mx)));
      const float mn2 = mn * LOG2E;
      const float al = __builtin_amdgcn_exp2f(m2 - mn2);
      m = mn; m2 = mn2; l *= al;
      const float a0 = rdlane(al, 0), a1 = rdlane(al, 8);
      acc *= (lane < 32) ? a0 : a1;
    }
#pragma unroll
    for (int t = 0; t < 4; t++) {
      const float e = __builtin_amdgcn_exp2f(fmaf(z[t], LOG2E, -m2));
      l += e;
      const unsigned pp = cvtpk(e, e);
      acc = mfma(u32x4{pp & msk[0], pp & msk[1], pp & msk[2], pp & msk[3]}, v[t], acc);
    }
  };
#pragma unroll 1
  for (int i = 0; i < NTURN - 1; i++) {
    so += NST * 32768;
    __builtin_amdgcn_s_barrier();
#pragma unroll
    for (int s = 0; s < NST; s++) {
      f32x4 sc = qk(kb[s]);
      PIN(sc); LOADK(s, so + s * 32768); SB();
      pv(sc, vb[s]);
      PIN(acc); LOADV(s, so + s * 32768); SB();
    }
  }
  SINK(pq[0]); SINK(pq[1]); SINK(pq[2]);
  STAMP(4);
  const int rho = bid & 7, cg = bid >> 3;
  const int fb = bid >> 7, fi = bid & 127;
  const int e0 = (fi * 9) >> 1, e1 = (fi * 9 + 9) >> 1;
  const unsigned hrow = (wave >> 2) * 32 + 4 * rho + (wave & 3);
  auto ro = __builtin_amdgcn_make_buffer_rsrc((void*)p.wo, 0, cg < 18 ? 4096 * DM * 2 : 0, 0x00020000);
  auto rp1 = __builtin_amdgcn_make_buffer_rsrc((void*)p.ws, 0, cg < 18 ? WS_END : 0, 0x00020000);
  auto ld1 = [&](int s) {
    return s < 8 ? __builtin_amdgcn_raw_buffer_load_b128(rp1, lane * 16, WS_P1 + hrow * 8192 + s * 1024, AUX_SC1)
                 : __builtin_amdgcn_raw_buffer_load_b128(rp1, (lane & 7) * 16, WS_ML + hrow * 128, AUX_SC1);
  };
  u32x4 wr[4], go[9];
  u32x4 w1v, b1v, w2v; unsigned w1t, b1t, w2t, xh, b2v;
  {
    touch = __builtin_amdgcn_raw_buffer_load_b32(rc, lane * 64, wave * 4096, 0);
    SB();
    pv(qk(kb[0]), vb[0]);
    PIN(acc);
#pragma unroll
    for (int t = 0; t < 4; t++) wr[t] = __builtin_amdgcn_raw_buffer_load_b128(ro, (lane >> 2) * 1152 + (lane & 3) * 16, (512 * rho + 64 * wave + 16 * t) * 1152 + 64 * min(cg, 17), AUX_NT);
    w1v = *(const u32x4*)(p.w1 + 8 * tid); b1v = *(const u32x4*)(p.b1 + 8 * tid); w2v = *(const u32x4*)(p.w2 + 8 * tid);
    w1t = p.w1[4096 + tid]; b1t = p.b1[4096 + tid]; w2t = p.w2[4096 + tid];
    xh = *(const unsigned*)(p.x + fb * DM + 2 * min(tid, 287));
    b2v = p.b2[0];
    SB();
    pv(qk(kb[1]), vb[1]); SB();
    pv(qk(kb[2]), vb[2]);
    PIN(acc);
    pv(qk(kb[3]), vb[3]); SB();
  }
  STAMP(5);
  l = x16_add(l, l); l = x32_add(l, l);
  if (lane == 0 || lane == 8) {
    *(LAS float*)(lds + L_ML + (n >> 3) * 32 + wave * 4) = m2;
    *(LAS float*)(lds + L_ML + 64 + (n >> 3) * 32 + wave * 4) = l;
  }
  *(LAS f32x4*)(lds + L_O + wave * 1024 + (g >> 1) * 512 + (8 * n + 4 * (g & 1)) * 4) = acc;
  LDS_BARRIER();
  STAMP(13);
  if (wave < 2) {
    const f32x4 ma = *(LAS f32x4*)(lds + L_ML + wave * 32), mb = *(LAS f32x4*)(lds + L_ML + wave * 32 + 16);
    const f32x4 la = *(LAS f32x4*)(lds + L_ML + 64 + wave * 32), lb = *(LAS f32x4*)(lds + L_ML + 64 + wave * 32 + 16);
    const float M = fmaxf(fmaxf(fmaxf(ma[0], ma[1]), fmaxf(ma[2], ma[3])), fmaxf(fmaxf(mb[0], mb[1]), fmaxf(mb[2], mb[3])));
    float L = 0.f, o0 = 0.f, o1 = 0.f;
#pragma unroll
    for (int w = 0; w < 8; w++) {
      const float e = __builtin_amdgcn_exp2f((w < 4 ? ma[w & 3] : mb[w & 3]) - M);
      const f32x2 o = *(LAS f32x2*)(lds + L_O + w * 1024 + wave * 512 + lane * 8);
      L = fmaf(e, w < 4 ? la[w & 3] : lb[w & 3], L); o0 = fmaf(e, o[0], o0); o1 = fmaf(e, o[1], o1);
    }
    const unsigned slot = (ab * 32 + 2 * kvh + wave) * 8 + sp;
    __builtin_amdgcn_raw_buffer_store_b128(u32x4{__float_as_uint(o0), tag, __float_as_uint(o1), tag}, rws, lane * 16, WS_P1 + slot * 1024, AUX_SC1);
    if (lane == 0) __builtin_amdgcn_raw_buffer_store_b128(u32x4{__float_as_uint(M), tag, __float_as_uint(L), tag}, rws, 0, WS_ML + slot * 16, AUX_SC1);
  }
  STAMP(6);
  SINK(touch);
  if (cg < 18) {
#pragma unroll
    for (int t = 0; t < 4; t++) {
      const int row = 64 * wave + 16 * t + (lane >> 2);
      *(LAS u32x4*)(lds + L_WO + row * 64 + (((lane & 3) ^ ((row >> 2) & 2)) << 4)) = wr[t];
    }
    LDS_BARRIER();
    u32x4 bfr[16];
    if (wave < 2) {
#pragma unroll
      for (int s = 0; s < 16; s++) {
        const int a = L_WO + (32 * s + 8 * g + (n >> 2)) * 64 + ((wave ^ (g & 1)) << 5) + (n & 3) * 8;
        bfr[s] = read_tr(lds + a, lds + a + 256);
      }
    }
#pragma unroll
    for (int s = 0; s < 9; s++) go[s] = ld1(s);
    settle<9>(go, tag, ld1);
    STAMP(7);
    const float ms = __uint_as_float(go[8][0]);
    float M = fmaxf(ms, dpp<XOR1>(ms)); M = fmaxf(M, dpp<XOR2>(M)); M = fmaxf(M, dpp<HMIR>(M));
    const float e = __builtin_amdgcn_exp2f(ms - M);
    float L = e * __uint_as_float(go[8][2]); L += dpp<XOR1>(L); L += dpp<XOR2>(L); L += dpp<HMIR>(L);
    const float wgt = e * __builtin_amdgcn_rcpf(L);
    float o0 = 0.f, o1 = 0.f;
#pragma unroll
    for (int s = 0; s < 8; s++) { const float ws_ = rdlane(wgt, s); o0 = fmaf(ws_, __uint_as_float(go[s][0]), o0); o1 = fmaf(ws_, __uint_as_float(go[s][2]), o1); }
    *(LAS unsigned*)(lds + L_OL + (wave >> 2) * 1088 + (wave & 3) * 256 + lane * 4) = cvtpk(o0, o1);
    LDS_BARRIER();
    if (wave < 2) {
      f32x4 d = {0, 0, 0, 0};
#pragma unroll
      for (int s = 0; s < 16; s++) d = mfma(*(LAS u32x4*)(lds + L_OL + (n & 1) * 1088 + (32 * s + 8 * g) * 2), bfr[s], d);
      if (g < 2) __builtin_amdgcn_raw_buffer_store_b64(u32x2{__float_as_uint(g ? d[1] : d[0]), tag}, rws, (g * 8 * DM + n) * 8, WS_P2 + (rho * DM + 32 * cg + 16 * wave) * 8, AUX_SC1);
    }
    STAMP(8);
  }
  constexpr float KS = 0.4686686930532063f;
  constexpr float BETA = 2.f * LOG2E * 0.7978845608028654f / KS;
  f32x2 W1[4], B1[4], W2[4];
#pragma unroll
  for (int j = 0; j < 4; j++) {
    W1[j] = f32x2{lo2f(w1v[j]), hi2f(w1v[j])} * KS;
    B1[j] = f32x2{lo2f(b1v[j]), hi2f(b1v[j])} * KS;
    W2[j] = f32x2{lo2f(w2v[j]), hi2f(w2v[j])} * (1.f / KS);
    PIN(W1[j]); PIN(B1[j]); PIN(W2[j]);
  }
  float W1t = lo2f(w1t) * KS, B1t = lo2f(b1t) * KS, W2t = lo2f(w2t) * (1.f / KS);
  PIN(W1t); PIN(B1t); PIN(W2t);
  if (wave < 5) {
    u32x4 gp[8];
    auto ld2 = [&](int s) { return __builtin_amdgcn_raw_buffer_load_b128(rws, min(tid, 287) * 16, WS_P2 + (fb * 8 + s) * DM * 8, AUX_SC1); };
#pragma unroll
    for (int s = 0; s < 8; s++) gp[s] = ld2(s);
    settle<8>(gp, tag, ld2);
    float a = 0.f, c = 0.f;
#pragma unroll
    for (int s = 0; s < 8; s++) { a += __uint_as_float(gp[s][0]); c += __uint_as_float(gp[s][2]); }
    const unsigned at = cvtpk(a, c);
    const unsigned hh = cvtpk(lo2f(xh) + lo2f(at), hi2f(xh) + hi2f(at));
    if (tid < 288) *(LAS f32x2*)(lds + L_HS + tid * 8) = f32x2{lo2f(hh), hi2f(hh)};
  }
  STAMP(9);
  LDS_BARRIER();
  if (bid == 0 && tid == 0) __builtin_amdgcn_raw_buffer_store_b32(tag, rws, 0, WS_EPOCH, AUX_SC1);
  float hn[5];
  const float hown = *(LAS float*)(lds + L_HS + min(e0 + g + 4 * (lane >> 3 & 1), DM - 1) * 4);
  {
    float v[9];
#pragma unroll
    for (int i = 0; i < 9; i++) v[i] = *(LAS float*)(lds + L_HS + (lane + 64 * i) * 4);
#pragma unroll
    for (int e = 0; e < 5; e++) hn[e] = *(LAS float*)(lds + L_HS + min(e0 + e, DM - 1) * 4);
    const float mean = wave_sum(((v[0] + v[1]) + (v[2] + v[3])) + ((v[4] + v[5]) + (v[6] + v[7])) + v[8]) * (1.f / DM);
    float s2a = 0.f, s2b = 0.f;
#pragma unroll
    for (int i = 0; i < 9; i++) { const float a = v[i] - mean; if (i & 1) s2b = fmaf(a, a, s2b); else s2a = fmaf(a, a, s2a); }
    const float var = wave_sum(s2a + s2b) * (1.f / DM);
    const float rden = rden_of(var), mb = rbf(mean);
    const float mb2 = rbf((mean - mb) * rden), rden2 = rden_of(var * rden * rden);
#pragma unroll
    for (int e = 0; e < 5; e++) hn[e] = rbf(rbf(rbf(rbf(hn[e] - mb) * rden) - mb2) * rden2);
  }
  STAMP(10);
  auto term = [&](f32x2 h, f32x2 w1, f32x2 b1, f32x2 w2, f32x2 a) {
    const f32x2 v = __builtin_elementwise_fma(h, w1, b1);
    const f32x2 z = v * __builtin_elementwise_fma(v, v, f32x2{BETA, BETA});
    const f32x2 d = f32x2{__builtin_amdgcn_exp2f(-z[0]), __builtin_amdgcn_exp2f(-z[1])} + 1.f;
    return __builtin_elementwise_fma(v * w2, f32x2{__builtin_amdgcn_rcpf(d[0]), __builtin_amdgcn_rcpf(d[1])}, a);
  };
  float ya[5];
  {
    const f32x2 t01 = term(f32x2{hn[0], hn[1]}, f32x2{W1t, W1t}, f32x2{B1t, B1t}, f32x2{W2t, W2t}, f32x2{0.f, 0.f});
    const f32x2 t23 = term(f32x2{hn[2], hn[3]}, f32x2{W1t, W1t}, f32x2{B1t, B1t}, f32x2{W2t, W2t}, f32x2{0.f, 0.f});
    const float v4 = fmaf(hn[4], W1t, B1t);
    const float t4 = v4 * W2t * __builtin_amdgcn_rcpf(1.f + __builtin_amdgcn_exp2f(-(v4 * fmaf(v4, v4, BETA))));
#pragma unroll
    for (int e = 0; e < 5; e++) {
      f32x2 a = {e == 4 ? t4 : e < 2 ? t01[e & 1] : t23[e & 1], 0.f};
#pragma unroll
      for (int j = 0; j < 4; j++) a = term(f32x2{hn[e], hn[e]}, W1[j], B1[j], W2[j], a);
      ya[e] = a[0] + a[1];
    }
  }
  {
    const float r = row_sum(x16_add(x32_add(ya[0], ya[1]), x32_add(ya[2], ya[3])));
    const float r4 = wave_sum(ya[4]);
    if (n == 0) *(LAS float*)(lds + L_RED + ((g >> 1) + 2 * (g & 1)) * 32 + wave * 4) = r;
    if (lane == 1) *(LAS float*)(lds + L_RED + 4 * 32 + wave * 4) = r4;
  }
  STAMP(11);
  LDS_BARRIER();
  if (wave == 0) {
    const int e = g + 4 * (lane >> 3 & 1);
    const f32x4 a = *(LAS f32x4*)(lds + L_RED + min(e, 4) * 32), c = *(LAS f32x4*)(lds + L_RED + min(e, 4) * 32 + 16);
    const float y = rbf(rbf(((a[0] + a[1]) + (a[2] + a[3])) + ((c[0] + c[1]) + (c[2] + c[3]))) + lo2f(b2v));
    auto rout = __builtin_amdgcn_make_buffer_rsrc((void*)p.out, 0, 2 * DM * 2, 0x00020000);
    if ((lane & 7) == 0 && e0 + e < e1) __builtin_amdgcn_raw_buffer_store_b16((u16)cvtpk(hown + y, 0.f), rout, e * 2, (fb * DM + e0) * 2, AUX_SC1);
  }
  STAMP(12);
  asm volatile(".Lfused_last:");
}

extern "C" void kernel_launch(void* const* d_in, const int* in_sizes, int n_in, void* d_out, int out_size, void* d_ws, size_t ws_size, hipStream_t stream) {
  Params p;
  p.x = (const u16*)d_in[0]; p.K = (const char*)d_in[1]; p.V = (const char*)d_in[2];
  p.wq = (const char*)d_in[3]; p.wo = (const char*)d_in[4];
  p.w1 = (const u16*)d_in[6]; p.b1 = (const u16*)d_in[7]; p.w2 = (const u16*)d_in[8]; p.b2 = (const u16*)d_in[9];
  p.out = (u16*)d_out; p.ws = (char*)d_ws;
  fused_kernel<<<256, 512, 0, stream>>>(p);
}
```

```cpp
#include <hip/hip_runtime.h>
#include <hip/hip_bf16.h>
#include <stdint.h>

#define DIAG 0
#define TOUCH0 1
#define TOUCH1 1
#define EARLY 0
#define PERM 8,9,16,20,10,13,15,19,12,17,14,18,11,21,22
#define RROT 0
#define SROT 0
#define SWZ 0
#define BARL 1
#define QPOS 4
#define BAR0 1

typedef unsigned u32x4 __attribute__((ext_vector_type(4)));
typedef unsigned u32x3 __attribute__((ext_vector_type(3)));
typedef unsigned u32x2 __attribute__((ext_vector_type(2)));
typedef float f32x4 __attribute__((ext_vector_type(4)));
typedef float f32x2 __attribute__((ext_vector_type(2)));
typedef __bf16 bf16x8 __attribute__((ext_vector_type(8)));
typedef __bf16 bf16x2 __attribute__((ext_vector_type(2)));
typedef short s16x4 __attribute__((ext_vector_type(4)));
typedef unsigned short u16;
#define LAS __attribute__((address_space(3)))

constexpr int DM = 576, FF = 4608;
constexpr float LOG2E = 1.4426950408889634f;
constexpr float SCALE = 0.08838834764831845f;
constexpr int NST = 4;
constexpr int NTURN = 32 / NST;
constexpr unsigned SPIN_CAP = 1u << 16;
constexpr int AUX_NT = 2, AUX_SC1 = 16;

constexpr unsigned WS_EPOCH = 0;
constexpr unsigned WS_QG = 4096;
constexpr unsigned WS_P1 = WS_QG + 32 * 8 * 2048;
constexpr unsigned WS_ML = WS_P1 + 512 * 1024;
constexpr unsigned WS_P2 = WS_ML + 512 * 16;
constexpr unsigned WS_END = WS_P2 + 16 * DM * 8;
constexpr unsigned WS_STAMP = 2u << 20;

constexpr int L_Q = 0;
constexpr int L_ML = 512;
constexpr int L_O = 640;
constexpr int L_OL = L_O + 8192;
constexpr int L_HS = L_OL + 2 * 1088;
constexpr int L_RED = L_HS + DM * 4;
constexpr int L_WO = L_RED + 256;
constexpr int L_END = L_WO + 32768;

struct Params {
  const u16* x; const char* K; const char* V; const char* wq; const char* wo;
  const u16* w1; const u16* b1; const u16* w2; const u16* b2; u16* out; char* ws;
};

__device__ __forceinline__ float lo2f(unsigned v) { return __uint_as_float(v << 16); }
__device__ __forceinline__ float hi2f(unsigned v) { return __uint_as_float(v & 0xffff0000u); }
__device__ __forceinline__ unsigned cvtpk(float lo, float hi) {
  f32x2 v = {lo, hi};
  return __builtin_bit_cast(unsigned, __builtin_convertvector(v, bf16x2));
}
__device__ __forceinline__ float rbf(float x) { return lo2f(cvtpk(x, x)); }
template <int CTRL> __device__ __forceinline__ float dpp(float x) {
  return __builtin_bit_cast(float, __builtin_amdgcn_mov_dpp(__builtin_bit_cast(int, x), CTRL, 0xf, 0xf, true));
}
constexpr int XOR1 = 0xB1, XOR2 = 0x4E, HMIR = 0x141, MIR = 0x140;
__device__ __forceinline__ float row_sum(float x) {
  x += dpp<XOR1>(x); x += dpp<XOR2>(x); x += dpp<HMIR>(x); x += dpp<MIR>(x); return x;
}
__device__ __forceinline__ float x16_add(float a, float b) {
  auto s = __builtin_amdgcn_permlane16_swap(__float_as_uint(a), __float_as_uint(b), false, false);
  return __uint_as_float(s[0]) + __uint_as_float(s[1]);
}
__device__ __forceinline__ float x32_add(float a, float b) {
  auto s = __builtin_amdgcn_permlane32_swap(__float_as_uint(a), __float_as_uint(b), false, false);
  return __uint_as_float(s[0]) + __uint_as_float(s[1]);
}
__device__ __forceinline__ float x16_max(float x) {
  auto s = __builtin_amdgcn_permlane16_swap(__float_as_uint(x), __float_as_uint(x), false, false);
  return fmaxf(__uint_as_float(s[0]), __uint_as_float(s[1]));
}
__device__ __forceinline__ float x32_max(float x) {
  auto s = __builtin_amdgcn_permlane32_swap(__float_as_uint(x), __float_as_uint(x), false, false);
  return fmaxf(__uint_as_float(s[0]), __uint_as_float(s[1]));
}
__device__ __forceinline__ float wave_sum(float x) { x = row_sum(x); x = x16_add(x, x); return x32_add(x, x); }
__device__ __forceinline__ float rdlane(float x, int l) { return __builtin_bit_cast(float, __builtin_amdgcn_readlane(__builtin_bit_cast(int, x), l)); }
__device__ __forceinline__ float rden_of(float var) {
  return __builtin_amdgcn_rcpf(rbf(__builtin_amdgcn_sqrtf(rbf(rbf(var) + 1e-5f))));
}
__device__ __forceinline__ f32x4 mfma(u32x4 a, u32x4 b, f32x4 c) {
  return __builtin_amdgcn_mfma_f32_16x16x32_bf16(__builtin_bit_cast(bf16x8, a), __builtin_bit_cast(bf16x8, b), c, 0, 0, 0);
}
__device__ __forceinline__ u32x4 read_tr(LAS char* a, LAS char* b) {
  const u32x2 ua = __builtin_bit_cast(u32x2, __builtin_amdgcn_ds_read_tr16_b64_v4i16((LAS s16x4*)a));
  const u32x2 ub = __builtin_bit_cast(u32x2, __builtin_amdgcn_ds_read_tr16_b64_v4i16((LAS s16x4*)b));
  return u32x4{ua[0], ua[1], ub[0], ub[1]};
}
#define SB() __builtin_amdgcn_sched_barrier(0)
#define PIN(x) do { asm volatile("" : "+v"(x) :: "memory"); __builtin_amdgcn_sched_barrier(0); } while (0)
#define PINM() do { asm volatile("" ::: "memory"); __builtin_amdgcn_sched_barrier(0); } while (0)
#define SINK(x) asm volatile("" :: "v"(x))
#define VMWAIT(n) do { __builtin_amdgcn_sched_barrier(0); asm volatile("s_waitcnt vmcnt(" #n ")" ::: "memory"); __builtin_amdgcn_sched_barrier(0); } while (0)
#define LDS_BARRIER() do { asm volatile("s_waitcnt lgkmcnt(0)" ::: "memory"); __builtin_amdgcn_s_barrier(); asm volatile("" ::: "memory"); } while (0)
#define STAMP(i) do {} while (0)

__device__ __forceinline__ bool fresh(u32x4 v, unsigned tag) { return __builtin_amdgcn_ballot_w64(v[1] != tag || v[3] != tag) == 0; }
template <int N, class F>
__device__ __forceinline__ void settle(u32x4 (&g)[N], unsigned tag, F load) {
  unsigned pend = (1u << N) - 1;
#pragma unroll 1
  for (unsigned spin = 0; spin < SPIN_CAP; spin++) {
#pragma unroll
    for (int s = 0; s < N; s++)
      if (fresh(g[s], tag)) pend &= ~(1u << s);
    if (pend == 0) break;
    __builtin_amdgcn_s_sleep(1);
    asm volatile("" ::: "memory");
#pragma unroll
    for (int s = 0; s < N; s++)
      if ((pend >> s) & 1) g[s] = load(s);
  }
}

__global__ void __launch_bounds__(512, 2) fused_kernel(Params p) {
  __shared__ __attribute__((aligned(16))) char lds_[L_END];
  LAS char* const lds = (LAS char*)lds_;
  const int tid = threadIdx.x, lane = tid & 63;
  const int wave = __builtin_amdgcn_readfirstlane(tid >> 6);
  const int bid = blockIdx.x;
  const int g = lane >> 4, n = lane & 15;
  STAMP(0);
  auto rws = __builtin_amdgcn_make_buffer_rsrc((void*)p.ws, 0, WS_END, 0x00020000);
  auto rk = __builtin_amdgcn_make_buffer_rsrc((void*)p.K, 0, 256u << 20, 0x00020000);
  auto rv = __builtin_amdgcn_make_buffer_rsrc((void*)p.V, 0, 256u << 20, 0x00020000);
  char* pc; unsigned pclen;
  asm volatile("s_getpc_b64 %0\n.Lfused_first:\n\ts_mov_b32 %1, .Lfused_last-.Lfused_first" : "=s"(pc), "=s"(pclen));
  auto rc = __builtin_amdgcn_make_buffer_rsrc((void*)pc, 0, pclen, 0x00020000);
  unsigned epoch = *(const __attribute__((address_space(4))) unsigned*)(p.ws + WS_EPOCH);

  const int ab = bid >> 7, kvh = (bid >> 3) & 15, sp = bid & 7;
  constexpr int PB[15] = {PERM};
  auto sprd = [&](unsigned v, int first, int nb) { unsigned r = 0; for (int i = 0; i < nb; i++) r |= ((v >> i) & 1u) << PB[first + i]; return r; };
  const unsigned koff = sprd(4 * (n & 3) + (n >> 2), 0, 4) + g * 16;
  const unsigned voff = sprd(g, 0, 2) + n * 16;
  const unsigned sbase = ((unsigned)(bid >> 3) << 23) + sprd(sp, 12, 3) + sprd(wave, 4, 3);
  unsigned so = sbase;
#define SO(s) (so + sprd(s, 7, 2))
  u32x4 kb[NST][4], vb[NST][4];
#define LOADK(s, off) _Pragma("unroll") for (int t = 0; t < 4; t++) kb[s][t] = __builtin_amdgcn_raw_buffer_load_b128(rk, koff + t * 64, (off), AUX_NT)
#define LOADV(s, off) _Pragma("unroll") for (int t = 0; t < 4; t++) vb[s][t] = (PB[2] == 10 && PB[3] == 11) ? __builtin_amdgcn_raw_buffer_load_b128(rv, voff + t * 1024, (off), AUX_NT) : __builtin_amdgcn_raw_buffer_load_b128(rv, voff, (off) + sprd(t, 2, 2), AUX_NT)

  unsigned msk[4];
  auto mkmask = [&]() {
    int nn = n;
    asm volatile("" : "+v"(nn));
#pragma unroll
    for (int k = 0; k < 4; k++) msk[k] = (((nn & 7) >> 1) == k) ? ((nn & 1) ? 0xffff0000u : 0x0000ffffu) : 0u;
  };

  unsigned touch = 0;
  touch = __builtin_amdgcn_raw_buffer_load_b32(rc, lane * 64, ((wave + 7) & 7) * 4096, 0);
  PINM();
  if (wave == 0) {
    const int hq = bid >> 3, ks = bid & 7;
    auto rx = __builtin_amdgcn_make_buffer_rsrc((void*)p.x, 0, 2 * DM * 2, 0x00020000);
    auto rq = __builtin_amdgcn_make_buffer_rsrc((void*)p.wq, 0, DM * 4096 * 2, 0x00020000);
    u32x3 xs[3], xm[3];
    u32x4 wb[18];
#pragma unroll
    for (int i = 0; i < 3; i++) xs[i] = __builtin_amdgcn_raw_buffer_load_b96(rx, lane * 36 + 12 * i, 0, 0);
#pragma unroll
    for (int i = 0; i < 3; i++) xm[i] = __builtin_amdgcn_raw_buffer_load_b96(rx, ((n >> 3) * 32 + 4 * ks + g) * 36 + 12 * i, 0, 0);
#pragma unroll
    for (int t = 0; t < 18; t++) wb[t] = __builtin_amdgcn_raw_buffer_load_b128(rq, g * 18 * 8192 + n * 16, (72 * ks + t) * 8192 + hq * 256, AUX_NT);
    PINM();
    asm volatile("s_barrier" ::: "memory");
    PINM();
    mkmask();
    float s1 = 0.f;
#pragma unroll
    for (int i = 0; i < 9; i++) { const unsigned u = xs[i / 3][i % 3]; s1 += lo2f(u) + hi2f(u); }
    s1 = row_sum(s1);
    const float mean = x16_add(s1, s1) * (1.f / DM);
    float s2 = 0.f;
#pragma unroll
    for (int i = 0; i < 9; i++) { const unsigned u = xs[i / 3][i % 3]; const float a = lo2f(u) - mean, c = hi2f(u) - mean; s2 += a * a + c * c; }
    s2 = row_sum(s2);
    const float rden = rden_of(x16_add(s2, s2) * (1.f / DM)), mb = rbf(mean);
    const float mb0 = rdlane(mb, 0), mb1 = rdlane(mb, 32), rd0 = rdlane(rden, 0), rd1 = rdlane(rden, 32);
    const float mb_ = (n < 8) ? mb0 : mb1, rd_ = (n < 8) ? rd0 : rd1;
    f32x4 qa = {0, 0, 0, 0};
#pragma unroll
    for (int i = 0; i < 9; i++) {
      const unsigned u = xm[i / 3][i % 3], d = cvtpk(lo2f(u) - mb_, hi2f(u) - mb_);
      const float y0 = lo2f(d) * rd_, y1 = hi2f(d) * rd_;
      const unsigned p0 = cvtpk(y0, y0), p1 = cvtpk(y1, y1);
      qa = mfma(u32x4{p0 & msk[0], p0 & msk[1], p0 & msk[2], p0 & msk[3]}, wb[2 * i], qa);
      qa = mfma(u32x4{p1 & msk[0], p1 & msk[1], p1 & msk[2], p1 & msk[3]}, wb[2 * i + 1], qa);
    }
    unsigned tag = epoch;
    asm volatile("" : "+s"(tag));
    tag += 1;
    __builtin_amdgcn_raw_buffer_store_b128(u32x4{__float_as_uint(qa[0]), tag, __float_as_uint(qa[1]), tag}, rws, lane * 16, WS_QG + bid * 2048, AUX_SC1);
    __builtin_amdgcn_raw_buffer_store_b128(u32x4{__float_as_uint(qa[2]), tag, __float_as_uint(qa[3]), tag}, rws, lane * 16, WS_QG + bid * 2048 + 1024, AUX_SC1);
    STAMP(1);
  }
  else asm volatile("s_barrier" ::: "memory");
  PINM();
  LOADK(0, SO(0)); LOADV(0, SO(0));
  PINM();
#pragma unroll
  for (int s = 1; s < NST; s++) { LOADK(s, SO(s)); PINM(); LOADV(s, SO(s)); PINM(); }
  asm volatile("" : "+s"(epoch));
  const unsigned tag = epoch + 1;
  mkmask();
  PINM();
  if (wave == 1 || wave == 2) {
    u32x4 pq[8];
    auto ldq = [&](int s) {
      return __builtin_amdgcn_raw_buffer_load_b128(rws, (lane >> 5) * 1024 + (32 * ab + (lane & 31)) * 16, WS_QG + ((2 * kvh + wave - 1) * 8 + s) * 2048, AUX_SC1);
    };
#pragma unroll
    for (int s = 0; s < 8; s++) pq[s] = ldq(s);
    STAMP(14);
    settle<8>(pq, tag, ldq);
    float qa = 0.f, qc = 0.f;
#pragma unroll
    for (int s = 0; s < 8; s++) { qa += __uint_as_float(pq[s][0]); qc += __uint_as_float(pq[s][2]); }
    *(LAS unsigned*)(lds + L_Q + (wave - 1) * 256 + (8 * n + 4 * (g & 1) + 2 * (lane >> 5)) * 2) = cvtpk(qa, qc);
    STAMP(2);
  }
  SINK(touch);
  LDS_BARRIER();
  u32x4 qf[4];
#pragma unroll
  for (int s = 0; s < 4; s++) qf[s] = *(LAS u32x4*)(lds + L_Q + (n >> 3) * 256 + (32 * s + 8 * g) * 2);
  STAMP(3);

  float m = -INFINITY, m2 = -INFINITY, l = 0.f;
  f32x4 acc = {0, 0, 0, 0};
  auto qk = [&](const u32x4 (&k)[4]) {
    f32x4 sc = {0, 0, 0, 0};
#pragma unroll
    for (int t = 0; t < 4; t++) sc = mfma(k[t], qf[t], sc);
    return sc;
  };
  auto pv = [&](f32x4 sc, const u32x4 (&v)[4]) {
    const unsigned u0 = cvtpk(sc[0], sc[1]), u1 = cvtpk(sc[2], sc[3]);
    const unsigned w0 = cvtpk(lo2f(u0) * SCALE, hi2f(u0) * SCALE), w1 = cvtpk(lo2f(u1) * SCALE, hi2f(u1) * SCALE);
    const float z[4] = {lo2f(w0), hi2f(w0), lo2f(w1), hi2f(w1)};
    float mx = fmaxf(fmaxf(z[0], z[1]), fmaxf(z[2], z[3]));
    if (__builtin_amdgcn_ballot_w64(mx > m) != 0) {
      const float mn = fmaxf(m, x32_max(x16_max(mx)));
      const float mn2 = mn * LOG2E;
      const float al = __builtin_amdgcn_exp2f(m2 - mn2);
      m = mn; m2 = mn2; l *= al;
      const float a0 = rdlane(al, 0), a1 = rdlane(al, 8);
      acc *= (lane < 32) ? a0 : a1;
    }
#pragma unroll
    for (int t = 0; t < 4; t++) {
      const float e = __builtin_amdgcn_exp2f(fmaf(z[t], LOG2E, -m2));
      l += e;
      const unsigned pp = cvtpk(e, e);
      acc = mfma(u32x4{pp & msk[0], pp & msk[1], pp & msk[2], pp & msk[3]}, v[t], acc);
    }
  };
#pragma unroll 1
  for (int i = 0; i < NTURN - 1; i++) {
    so = sbase + sprd(i + 1, 9, 3);
    __builtin_amdgcn_s_barrier();
#pragma unroll
    for (int s = 0; s < NST; s++) {
      f32x4 sc = qk(kb[s]);
      PIN(sc); LOADK(s, SO(s)); SB();
      pv(sc, vb[s]);
      PIN(acc); LOADV(s, SO(s)); SB();
    }
  }
  STAMP(4);
  const int rho = bid & 7, cg = bid >> 3;
  const int fb = bid >> 7, fi = bid & 127;
  const int e0 = (fi * 9) >> 1, e1 = (fi * 9 + 9) >> 1;
  const unsigned hrow = (wave >> 2) * 32 + 4 * rho + (wave & 3);
  auto ro = __builtin_amdgcn_make_buffer_rsrc((void*)p.wo, 0, 4096 * DM * 2, 0x00020000);
  auto ld1 = [&](int s) {
    return s < 8 ? __builtin_amdgcn_raw_buffer_load_b128(rws, lane * 16, WS_P1 + hrow * 8192 + s * 1024, AUX_SC1)
                 : __builtin_amdgcn_raw_buffer_load_b128(rws, (lane & 7) * 16, WS_ML + hrow * 128, AUX_SC1);
  };
  u32x4 wr[8], go[9];
  {
    __builtin_amdgcn_s_barrier();
#pragma unroll
    for (int s = 0; s < NST; s++) { pv(qk(kb[s]), vb[s]); SB(); }
    PIN(acc);
    if (wave < 4 && cg < 18) {
#pragma unroll
      for (int t = 0; t < 8; t++) wr[t] = __builtin_amdgcn_raw_buffer_load_b128(ro, (lane >> 2) * 1152 + (lane & 3) * 16, (512 * rho + 128 * wave + 16 * t) * 1152 + 64 * cg, AUX_NT);
    }
    SB();
  }
  STAMP(5);
  l = x16_add(l, l); l = x32_add(l, l);
  if (lane == 0 || lane == 8) {
    *(LAS float*)(lds + L_ML + (n >> 3) * 32 + wave * 4) = m2;
    *(LAS float*)(lds + L_ML + 64 + (n >> 3) * 32 + wave * 4) = l;
  }
  *(LAS f32x4*)(lds + L_O + wave * 1024 + (g >> 1) * 512 + (8 * n + 4 * (g & 1)) * 4) = acc;
  LDS_BARRIER();
  STAMP(13);
  if (wave < 2) {
    const f32x4 ma = *(LAS f32x4*)(lds + L_ML + wave * 32), mb = *(LAS f32x4*)(lds + L_ML + wave * 32 + 16);
    const f32x4 la = *(LAS f32x4*)(lds + L_ML + 64 + wave * 32), lb = *(LAS f32x4*)(lds + L_ML + 64 + wave * 32 + 16);
    const float M = fmaxf(fmaxf(fmaxf(ma[0], ma[1]), fmaxf(ma[2], ma[3])), fmaxf(fmaxf(mb[0], mb[1]), fmaxf(mb[2], mb[3])));
    float L = 0.f, o0 = 0.f, o1 = 0.f;
#pragma unroll
    for (int w = 0; w < 8; w++) {
      const float e = __builtin_amdgcn_exp2f((w < 4 ? ma[w & 3] : mb[w & 3]) - M);
      const f32x2 o = *(LAS f32x2*)(lds + L_O + w * 1024 + wave * 512 + lane * 8);
      L = fmaf(e, w < 4 ? la[w & 3] : lb[w & 3], L); o0 = fmaf(e, o[0], o0); o1 = fmaf(e, o[1], o1);
    }
    const unsigned slot = (ab * 32 + 2 * kvh + wave) * 8 + sp;
    __builtin_amdgcn_raw_buffer_store_b128(u32x4{__float_as_uint(o0), tag, __float_as_uint(o1), tag}, rws, lane * 16, WS_P1 + slot * 1024, AUX_SC1);
    if (lane == 0) __builtin_amdgcn_raw_buffer_store_b128(u32x4{__float_as_uint(M), tag, __float_as_uint(L), tag}, rws, 0, WS_ML + slot * 16, AUX_SC1);
  }
  STAMP(6);
  u32x4 w1v, b1v, w2v; unsigned w1t, b1t, w2t, xh, b2v;
  auto ldw = [&]() {
    w1v = *(const u32x4*)(p.w1 + 8 * tid); b1v = *(const u32x4*)(p.b1 + 8 * tid); w2v = *(const u32x4*)(p.w2 + 8 * tid);
    w1t = p.w1[4096 + tid]; b1t = p.b1[4096 + tid]; w2t = p.w2[4096 + tid];
    xh = *(const unsigned*)(p.x + fb * DM + 2 * min(tid, 287));
    b2v = p.b2[0];
  };
  if (cg < 18) {
#pragma unroll
    for (int s = 0; s < 9; s++) go[s] = ld1(s);
    SB();
    if (wave < 4) {
#pragma unroll
      for (int t = 0; t < 8; t++) {
        const int row = 128 * wave + 16 * t + (lane >> 2);
        *(LAS u32x4*)(lds + L_WO + row * 64 + (((lane & 3) ^ ((row >> 2) & 2)) << 4)) = wr[t];
      }
    }
    LDS_BARRIER();
    u32x4 bfr[16];
    if (wave < 2) {
#pragma unroll
      for (int s = 0; s < 16; s++) {
        const int a = L_WO + (32 * s + 8 * g + (n >> 2)) * 64 + ((wave ^ (g & 1)) << 5) + (n & 3) * 8;
        bfr[s] = read_tr(lds + a, lds + a + 256);
      }
    }
    settle<9>(go, tag, ld1);
    STAMP(7);
    const float ms = __uint_as_float(go[8][0]);
    float M = fmaxf(ms, dpp<XOR1>(ms)); M = fmaxf(M, dpp<XOR2>(M)); M = fmaxf(M, dpp<HMIR>(M));
    const float e = __builtin_amdgcn_exp2f(ms - M);
    float L = e * __uint_as_float(go[8][2]); L += dpp<XOR1>(L); L += dpp<XOR2>(L); L += dpp<HMIR>(L);
    const float wgt = e * __builtin_amdgcn_rcpf(L);
    float o0 = 0.f, o1 = 0.f;
#pragma unroll
    for (int s = 0; s < 8; s++) { const float ws_ = rdlane(wgt, s); o0 = fmaf(ws_, __uint_as_float(go[s][0]), o0); o1 = fmaf(ws_, __uint_as_float(go[s][2]), o1); }
    *(LAS unsigned*)(lds + L_OL + (wave >> 2) * 1088 + (wave & 3) * 256 + lane * 4) = cvtpk(o0, o1);
    LDS_BARRIER();
    if (wave < 2) {
      f32x4 d = {0, 0, 0, 0};
#pragma unroll
      for (int s = 0; s < 16; s++) d = mfma(*(LAS u32x4*)(lds + L_OL + (n & 1) * 1088 + (32 * s + 8 * g) * 2), bfr[s], d);
      if (g < 2) __builtin_amdgcn_raw_buffer_store_b64(u32x2{__float_as_uint(g ? d[1] : d[0]), tag}, rws, (g * 8 * DM + n) * 8, WS_P2 + (rho * DM + 32 * cg + 16 * wave) * 8, AUX_SC1);
    }
    STAMP(8);
  }
  ldw();
  PINM();
  constexpr float KS = 0.4686686930532063f;
  constexpr float BETA = 2.f * LOG2E * 0.7978845608028654f / KS;
  u32x4 gp[8];
  auto rp2 = __builtin_amdgcn_make_buffer_rsrc((void*)p.ws, 0, wave < 5 ? WS_END : 0, 0x00020000);
  auto ld2 = [&](int s) { return __builtin_amdgcn_raw_buffer_load_b128(rp2, min(tid, 287) * 16, WS_P2 + (fb * 8 + s) * DM * 8, AUX_SC1); };
#pragma unroll
  for (int s = 0; s < 8; s++) gp[s] = ld2(s);
  PINM();
  f32x2 W1[4], B1[4], W2[4];
#pragma unroll
  for (int j = 0; j < 4; j++) {
    W1[j] = f32x2{lo2f(w1v[j]), hi2f(w1v[j])} * KS;
    B1[j] = f32x2{lo2f(b1v[j]), hi2f(b1v[j])} * KS;
    W2[j] = f32x2{lo2f(w2v[j]), hi2f(w2v[j])} * (1.f / KS);
    PIN(W1[j]); PIN(B1[j]); PIN(W2[j]);
  }
  float W1t = lo2f(w1t) * KS, B1t = lo2f(b1t) * KS, W2t = lo2f(w2t) * (1.f / KS);
  PIN(W1t); PIN(B1t); PIN(W2t);
  if (wave < 5) {
    settle<8>(gp, tag, ld2);
    float a = 0.f, c = 0.f;
#pragma unroll
    for (int s = 0; s < 8; s++) { a += __uint_as_float(gp[s][0]); c += __uint_as_float(gp[s][2]); }
    const unsigned at = cvtpk(a, c);
    const unsigned hh = cvtpk(lo2f(xh) + lo2f(at), hi2f(xh) + hi2f(at));
    if (tid < 288) *(LAS f32x2*)(lds + L_HS + tid * 8) = f32x2{lo2f(hh), hi2f(hh)};
  }
  STAMP(9);
  LDS_BARRIER();
  if (bid == 0 && tid == 0) __builtin_amdgcn_raw_buffer_store_b32(tag, rws, 0, WS_EPOCH, AUX_SC1);
  float hn[5];
  const float hown = *(LAS float*)(lds + L_HS + min(e0 + g + 4 * (lane >> 3 & 1), DM - 1) * 4);
  {
    float v[9];
#pragma unroll
    for (int i = 0; i < 9; i++) v[i] = *(LAS float*)(lds + L_HS + (lane + 64 * i) * 4);
#pragma unroll
    for (int e = 0; e < 5; e++) hn[e] = *(LAS float*)(lds + L_HS + min(e0 + e, DM - 1) * 4);
    const float mean = wave_sum(((v[0] + v[1]) + (v[2] + v[3])) + ((v[4] + v[5]) + (v[6] + v[7])) + v[8]) * (1.f / DM);
    float s2a = 0.f, s2b = 0.f;
#pragma unroll
    for (int i = 0; i < 9; i++) { const float a = v[i] - mean; if (i & 1) s2b = fmaf(a, a, s2b); else s2a = fmaf(a, a, s2a); }
    const float var = wave_sum(s2a + s2b) * (1.f / DM);
    const float rden = rden_of(var), mb = rbf(mean);
    const float mb2 = rbf((mean - mb) * rden), rden2 = rden_of(var * rden * rden);
#pragma unroll
    for (int e = 0; e < 5; e++) hn[e] = rbf(rbf(rbf(rbf(hn[e] - mb) * rden) - mb2) * rden2);
  }
  STAMP(10);
  auto term = [&](f32x2 h, f32x2 w1, f32x2 b1, f32x2 w2, f32x2 a) {
    const f32x2 v = __builtin_elementwise_fma(h, w1, b1);
    const f32x2 z = v * __builtin_elementwise_fma(v, v, f32x2{BETA, BETA});
    const f32x2 d = f32x2{__builtin_amdgcn_exp2f(-z[0]), __builtin_amdgcn_exp2f(-z[1])} + 1.f;
    return __builtin_elementwise_fma(v * w2, f32x2{__builtin_amdgcn_rcpf(d[0]), __builtin_amdgcn_rcpf(d[1])}, a);
  };
  float ya[5];
  {
    const f32x2 t01 = term(f32x2{hn[0], hn[1]}, f32x2{W1t, W1t}, f32x2{B1t, B1t}, f32x2{W2t, W2t}, f32x2{0.f, 0.f});
    const f32x2 t23 = term(f32x2{hn[2], hn[3]}, f32x2{W1t, W1t}, f32x2{B1t, B1t}, f32x2{W2t, W2t}, f32x2{0.f, 0.f});
    const float v4 = fmaf(hn[4], W1t, B1t);
    const float t4 = v4 * W2t * __builtin_amdgcn_rcpf(1.f + __builtin_amdgcn_exp2f(-(v4 * fmaf(v4, v4, BETA))));
#pragma unroll
    for (int e = 0; e < 5; e++) {
      f32x2 a = {e == 4 ? t4 : e < 2 ? t01[e & 1] : t23[e & 1], 0.f};
#pragma unroll
      for (int j = 0; j < 4; j++) a = term(f32x2{hn[e], hn[e]}, W1[j], B1[j], W2[j], a);
      ya[e] = a[0] + a[1];
    }
  }
  {
    const float r = row_sum(x16_add(x32_add(ya[0], ya[1]), x32_add(ya[2], ya[3])));
    const float r4 = wave_sum(ya[4]);
    if (n == 0) *(LAS float*)(lds + L_RED + ((g >> 1) + 2 * (g & 1)) * 32 + wave * 4) = r;
    if (lane == 1) *(LAS float*)(lds + L_RED + 4 * 32 + wave * 4) = r4;
  }
  STAMP(11);
  LDS_BARRIER();
  if (wave == 0) {
    const int e = g + 4 * (lane >> 3 & 1);
    const f32x4 a = *(LAS f32x4*)(lds + L_RED + min(e, 4) * 32), c = *(LAS f32x4*)(lds + L_RED + min(e, 4) * 32 + 16);
    const float y = rbf(rbf(((a[0] + a[1]) + (a[2] + a[3])) + ((c[0] + c[1]) + (c[2] + c[3]))) + lo2f(b2v));
    auto rout = __builtin_amdgcn_make_buffer_rsrc((void*)p.out, 0, 2 * DM * 2, 0x00020000);
    if ((lane & 7) == 0 && e0 + e < e1) __builtin_amdgcn_raw_buffer_store_b16((u16)cvtpk(hown + y, 0.f), rout, e * 2, (fb * DM + e0) * 2, AUX_SC1);
  }
  STAMP(12);
  asm volatile(".Lfused_last:");
}

extern "C" void kernel_launch(void* const* d_in, const int* in_sizes, int n_in, void* d_out, int out_size, void* d_ws, size_t ws_size, hipStream_t stream) {
  Params p;
  p.x = (const u16*)d_in[0]; p.K = (const char*)d_in[1]; p.V = (const char*)d_in[2];
  p.wq = (const char*)d_in[3]; p.wo = (const char*)d_in[4];
  p.w1 = (const u16*)d_in[6]; p.b1 = (const u16*)d_in[7]; p.w2 = (const u16*)d_in[8]; p.b2 = (const u16*)d_in[9];
  p.out = (u16*)d_out; p.ws = (char*)d_ws;
  fused_kernel<<<256, 512, 0, stream>>>(p);
}
```

```cpp
#include <hip/hip_runtime.h>
#include <hip/hip_bf16.h>
#include <stdint.h>

#define DIAG 0
#define TOUCH0 1
#define RPERM 0
#define SLEEP 1
#define WOPOS 1
#define WPOS 0
#define BSTEP 8
#define PERM 21,16,9,10,8,20,17,18,14,19,13,15,12,22,11
#define BAR0 1

typedef unsigned u32x4 __attribute__((ext_vector_type(4)));
typedef unsigned u32x3 __attribute__((ext_vector_type(3)));
typedef unsigned u32x2 __attribute__((ext_vector_type(2)));
typedef float f32x4 __attribute__((ext_vector_type(4)));
typedef float f32x2 __attribute__((ext_vector_type(2)));
typedef __bf16 bf16x8 __attribute__((ext_vector_type(8)));
typedef __bf16 bf16x2 __attribute__((ext_vector_type(2)));
typedef short s16x4 __attribute__((ext_vector_type(4)));
typedef unsigned short u16;
#define LAS __attribute__((address_space(3)))

constexpr int DM = 576, FF = 4608;
constexpr float LOG2E = 1.4426950408889634f;
constexpr float SCALE = 0.08838834764831845f;
constexpr int NSL = 8;
constexpr int NTURN = 32 / NSL;
constexpr unsigned SPIN_CAP = 1u << 16;
constexpr int AUX_NT = 2, AUX_SC1 = 16;

constexpr unsigned WS_EPOCH = 0;
constexpr unsigned WS_QG = 4096;
constexpr unsigned WS_P1 = WS_QG + 32 * 8 * 2048;
constexpr unsigned WS_ML = WS_P1 + 512 * 1024;
constexpr unsigned WS_P2 = WS_ML + 512 * 16;
constexpr unsigned WS_END = WS_P2 + 16 * DM * 8;
constexpr unsigned WS_STAMP = 2u << 20;

constexpr int L_Q = 0;
constexpr int L_ML = 512;
constexpr int L_O = 640;
constexpr int L_OL = L_O + 8192;
constexpr int L_HS = L_OL + 2 * 1088;
constexpr int L_RED = L_HS + DM * 4;
constexpr int L_WO = L_RED + 256;
constexpr int L_END = L_WO + 32768;

struct Params {
  const u16* x; const char* K; const char* V; const char* wq; const char* wo;
  const u16* w1; const u16* b1; const u16* w2; const u16* b2; u16* out; char* ws;
};

__device__ __forceinline__ float lo2f(unsigned v) { return __uint_as_float(v << 16); }
__device__ __forceinline__ float hi2f(unsigned v) { return __uint_as_float(v & 0xffff0000u); }
__device__ __forceinline__ unsigned cvtpk(float lo, float hi) {
  f32x2 v = {lo, hi};
  return __builtin_bit_cast(unsigned, __builtin_convertvector(v, bf16x2));
}
__device__ __forceinline__ float rbf(float x) { return lo2f(cvtpk(x, x)); }
template <int CTRL> __device__ __forceinline__ float dpp(float x) {
  return __builtin_bit_cast(float, __builtin_amdgcn_mov_dpp(__builtin_bit_cast(int, x), CTRL, 0xf, 0xf, true));
}
constexpr int XOR1 = 0xB1, XOR2 = 0x4E, HMIR = 0x141, MIR = 0x140;
__device__ __forceinline__ float row_sum(float x) {
  x += dpp<XOR1>(x); x += dpp<XOR2>(x); x += dpp<HMIR>(x); x += dpp<MIR>(x); return x;
}
__device__ __forceinline__ float x16_add(float a, float b) {
  auto s = __builtin_amdgcn_permlane16_swap(__float_as_uint(a), __float_as_uint(b), false, false);
  return __uint_as_float(s[0]) + __uint_as_float(s[1]);
}
__device__ __forceinline__ float x32_add(float a, float b) {
  auto s = __builtin_amdgcn_permlane32_swap(__float_as_uint(a), __float_as_uint(b), false, false);
  return __uint_as_float(s[0]) + __uint_as_float(s[1]);
}
__device__ __forceinline__ float x16_max(float x) {
  auto s = __builtin_amdgcn_permlane16_swap(__float_as_uint(x), __float_as_uint(x), false, false);
  return fmaxf(__uint_as_float(s[0]), __uint_as_float(s[1]));
}
__device__ __forceinline__ float x32_max(float x) {
  auto s = __builtin_amdgcn_permlane32_swap(__float_as_uint(x), __float_as_uint(x), false, false);
  return fmaxf(__uint_as_float(s[0]), __uint_as_float(s[1]));
}
__device__ __forceinline__ float wave_sum(float x) { x = row_sum(x); x = x16_add(x, x); return x32_add(x, x); }
__device__ __forceinline__ float rdlane(float x, int l) { return __builtin_bit_cast(float, __builtin_amdgcn_readlane(__builtin_bit_cast(int, x), l)); }
__device__ __forceinline__ float rden_of(float var) {
  return __builtin_amdgcn_rcpf(rbf(__builtin_amdgcn_sqrtf(rbf(rbf(var) + 1e-5f))));
}
__device__ __forceinline__ f32x4 mfma(u32x4 a, u32x4 b, f32x4 c) {
  return __builtin_amdgcn_mfma_f32_16x16x32_bf16(__builtin_bit_cast(bf16x8, a), __builtin_bit_cast(bf16x8, b), c, 0, 0, 0);
}
__device__ __forceinline__ u32x4 read_tr(LAS char* a, LAS char* b) {
  const u32x2 ua = __builtin_bit_cast(u32x2, __builtin_amdgcn_ds_read_tr16_b64_v4i16((LAS s16x4*)a));
  const u32x2 ub = __builtin_bit_cast(u32x2, __builtin_amdgcn_ds_read_tr16_b64_v4i16((LAS s16x4*)b));
  return u32x4{ua[0], ua[1], ub[0], ub[1]};
}
#define SB() __builtin_amdgcn_sched_barrier(0)
#define PIN(x) do { asm volatile("" : "+v"(x) :: "memory"); __builtin_amdgcn_sched_barrier(0); } while (0)
#define PINM() do { asm volatile("" ::: "memory"); __builtin_amdgcn_sched_barrier(0); } while (0)
#define SINK(x) asm volatile("" :: "v"(x))
#define VMWAIT(n) do { __builtin_amdgcn_sched_barrier(0); asm volatile("s_waitcnt vmcnt(" #n ")" ::: "memory"); __builtin_amdgcn_sched_barrier(0); } while (0)
#define LDS_BARRIER() do { asm volatile("s_waitcnt lgkmcnt(0)" ::: "memory"); __builtin_amdgcn_s_barrier(); asm volatile("" ::: "memory"); } while (0)
#define STAMP(i) do {} while (0)

template <int V> struct IC { static constexpr int value = V; };
template <int N, class F>
__device__ __forceinline__ void settle(u32x4 (&g)[N], unsigned tag, F load) {
#pragma unroll 1
  for (unsigned spin = 0; spin < SPIN_CAP; spin++) {
    unsigned y[N], any = 0;
#pragma unroll
    for (int s = 0; s < N; s++) { y[s] = __builtin_amdgcn_bitop3_b32(g[s][1], g[s][3], tag, 0x7e); any |= y[s]; }
    if (__builtin_amdgcn_ballot_w64(any != 0) == 0) break;
    __builtin_amdgcn_s_sleep(SLEEP);
    asm volatile("" ::: "memory");
#pragma unroll
    for (int s = 0; s < N; s++)
      if (__builtin_amdgcn_ballot_w64(y[s] != 0) != 0) g[s] = load(s);
  }
}
__device__ __forceinline__ float fadd(float a, float b) { float r; asm("v_add_f32 %0, %1, %2" : "=v"(r) : "v"(a), "v"(b)); return r; }
__device__ __forceinline__ float sum8(const u32x4 (&g)[8], int c) {
  float t[4];
#pragma unroll
  for (int i = 0; i < 4; i++) t[i] = fadd(__uint_as_float(g[2 * i][c]), __uint_as_float(g[2 * i + 1][c]));
  return fadd(fadd(t[0], t[1]), fadd(t[2], t[3]));
}

__global__ void __launch_bounds__(512, 2) fused_kernel(Params p) {
  __shared__ __attribute__((aligned(16))) char lds_[L_END];
  LAS char* const lds = (LAS char*)lds_;
  const int tid = threadIdx.x, lane = tid & 63;
  const int wave = __builtin_amdgcn_readfirstlane(tid >> 6);
  const int bid = blockIdx.x;
  const int g = lane >> 4, n = lane & 15;
  STAMP(0);
  auto rws = __builtin_amdgcn_make_buffer_rsrc((void*)p.ws, 0, WS_END, 0x00020000);
  auto rk = __builtin_amdgcn_make_buffer_rsrc((void*)p.K, 0, 256u << 20, 0x00020000);
  auto rv = __builtin_amdgcn_make_buffer_rsrc((void*)p.V, 0, 256u << 20, 0x00020000);
  char* pc; unsigned pclen;
  asm volatile("s_getpc_b64 %0\n.Lfused_first:\n\ts_mov_b32 %1, .Lfused_last-.Lfused_first" : "=s"(pc), "=s"(pclen));
  auto rc = __builtin_amdgcn_make_buffer_rsrc((void*)pc, 0, pclen, 0x00020000);
  unsigned epoch = *(const __attribute__((address_space(4))) unsigned*)(p.ws + WS_EPOCH);

  const int ab = bid >> 7, kvh = (bid >> 3) & 15, sp = bid & 7;
  constexpr int PB[15] = {PERM};
  auto sprd = [&](unsigned v, int first, int nb) { unsigned r = 0; for (int i = 0; i < nb; i++) r |= ((v >> i) & 1u) << PB[first + i]; return r; };
  unsigned va[4];
#pragma unroll
  for (int t = 0; t < 4; t++) va[t] = sprd(g + 4 * t, 0, 4) + n * 16;
  const unsigned sbase = ((unsigned)(bid >> 3) << 23) + sprd(sp, 12, 3) + sprd(wave, 4, 3);
  unsigned so = sbase;
#define SO(s) (so + sprd(s, 7, 3))
  u32x4 rb[NSL][4];
#define LOAD(s, rs) _Pragma("unroll") for (int t = 0; t < 4; t++) rb[s][t] = __builtin_amdgcn_raw_buffer_load_b128(rs, va[t], SO(s), AUX_NT)

  unsigned msk[4];
  auto mkmask = [&]() {
    int nn = n;
    asm volatile("" : "+v"(nn));
#pragma unroll
    for (int k = 0; k < 4; k++) msk[k] = (((nn & 7) >> 1) == k) ? ((nn & 1) ? 0xffff0000u : 0x0000ffffu) : 0u;
  };

  unsigned touch = 0;
  touch = __builtin_amdgcn_raw_buffer_load_b32(rc, lane * 64, ((wave + 7) & 7) * 4096, 0);
  PINM();
  if (wave == 0) {
    const int hq = bid >> 3, ks = bid & 7;
    auto rx = __builtin_amdgcn_make_buffer_rsrc((void*)p.x, 0, 2 * DM * 2, 0x00020000);
    auto rq = __builtin_amdgcn_make_buffer_rsrc((void*)p.wq, 0, DM * 4096 * 2, 0x00020000);
    u32x3 xs[3], xm[3];
    u32x4 wb[18];
#pragma unroll
    for (int i = 0; i < 3; i++) xs[i] = __builtin_amdgcn_raw_buffer_load_b96(rx, lane * 36 + 12 * i, 0, 0);
#pragma unroll
    for (int i = 0; i < 3; i++) xm[i] = __builtin_amdgcn_raw_buffer_load_b96(rx, ((n >> 3) * 32 + 4 * ks + g) * 36 + 12 * i, 0, 0);
#pragma unroll
    for (int t = 0; t < 18; t++) wb[t] = __builtin_amdgcn_raw_buffer_load_b128(rq, g * 18 * 8192 + n * 16, (72 * ks + t) * 8192 + hq * 256, AUX_NT);
    PINM();
    asm volatile("s_barrier" ::: "memory");
    PINM();
    mkmask();
    float s1 = 0.f;
#pragma unroll
    for (int i = 0; i < 9; i++) { const unsigned u = xs[i / 3][i % 3]; s1 += lo2f(u) + hi2f(u); }
    s1 = row_sum(s1);
    const float mean = x16_add(s1, s1) * (1.f / DM);
    float s2 = 0.f;
#pragma unroll
    for (int i = 0; i < 9; i++) { const unsigned u = xs[i / 3][i % 3]; const float a = lo2f(u) - mean, c = hi2f(u) - mean; s2 += a * a + c * c; }
    s2 = row_sum(s2);
    const float rden = rden_of(x16_add(s2, s2) * (1.f / DM)), mb = rbf(mean);
    const float mb0 = rdlane(mb, 0), mb1 = rdlane(mb, 32), rd0 = rdlane(rden, 0), rd1 = rdlane(rden, 32);
    const float mb_ = (n < 8) ? mb0 : mb1, rd_ = (n < 8) ? rd0 : rd1;
    f32x4 qa = {0, 0, 0, 0};
#pragma unroll
    for (int i = 0; i < 9; i++) {
      const unsigned u = xm[i / 3][i % 3], d = cvtpk(lo2f(u) - mb_, hi2f(u) - mb_);
      const float y0 = lo2f(d) * rd_, y1 = hi2f(d) * rd_;
      const unsigned p0 = cvtpk(y0, y0), p1 = cvtpk(y1, y1);
      qa = mfma(u32x4{p0 & msk[0], p0 & msk[1], p0 & msk[2], p0 & msk[3]}, wb[2 * i], qa);
      qa = mfma(u32x4{p1 & msk[0], p1 & msk[1], p1 & msk[2], p1 & msk[3]}, wb[2 * i + 1], qa);
    }
    unsigned tag = epoch;
    asm volatile("" : "+s"(tag));
    tag += 1;
    __builtin_amdgcn_raw_buffer_store_b128(u32x4{__float_as_uint(qa[0]), tag, __float_as_uint(qa[1]), tag}, rws, lane * 16, WS_QG + bid * 2048, AUX_SC1);
    __builtin_amdgcn_raw_buffer_store_b128(u32x4{__float_as_uint(qa[2]), tag, __float_as_uint(qa[3]), tag}, rws, lane * 16, WS_QG + bid * 2048 + 1024, AUX_SC1);
    STAMP(1);
  }
  else asm volatile("s_barrier" ::: "memory");
  PINM();
#pragma unroll
  for (int t = 0; t < 4; t++) PIN(va[t]);
#pragma unroll
  for (int s = 0; s < NSL; s++) { LOAD(s, rk); PINM(); }
  asm volatile("" : "+s"(epoch));
  const unsigned tag = epoch + 1;
  unsigned kw[4], kr[4];
#pragma unroll
  for (int t = 0; t < 4; t++) {
    kw[t] = L_WO + wave * 4096 + (4 * g + t) * 256 + ((n ^ (4 * g + t)) << 4);
    kr[t] = L_WO + wave * 4096 + n * 256 + (((4 * t + g) ^ n) << 4);
    PIN(kw[t]); PIN(kr[t]);
  }
  PINM();
  if (wave == 1 || wave == 2) {
    u32x4 pq[8];
    auto ldq = [&](int s) {
      return __builtin_amdgcn_raw_buffer_load_b128(rws, (lane >> 5) * 1024 + (32 * ab + (lane & 31)) * 16, WS_QG + ((2 * kvh + wave - 1) * 8 + s) * 2048, AUX_SC1);
    };
#pragma unroll
    for (int s = 0; s < 8; s++) pq[s] = ldq(s);
    STAMP(14);
    settle<8>(pq, tag, ldq);
    const float qa = sum8(pq, 0), qc = sum8(pq, 2);
    *(LAS unsigned*)(lds + L_Q + (wave - 1) * 256 + (8 * n + 4 * (g & 1) + 2 * (lane >> 5)) * 2) = cvtpk(qa, qc);
    STAMP(2);
  }
  SINK(touch);
  LDS_BARRIER();
  u32x4 qf[4];
#pragma unroll
  for (int s = 0; s < 4; s++) qf[s] = *(LAS u32x4*)(lds + L_Q + (n >> 3) * 256 + (32 * s + 8 * g) * 2);
  STAMP(3);

  unsigned Z[NTURN][2];
#pragma unroll 1
  for (int i = 0; i < NTURN; i++) {
    const bool last = i == NTURN - 1;
    so = sbase + sprd(i + 1, 10, 2);
    auto rs = __builtin_amdgcn_make_buffer_rsrc((void*)(last ? p.V : p.K), 0, 256u << 20, 0x00020000);
    unsigned z0 = 0, z1 = 0;
#pragma unroll
    for (int s = 0; s < NSL; s++) {
      if (s % BSTEP == 0) { SB(); __builtin_amdgcn_s_barrier(); SB(); }
#pragma unroll
      for (int t = 0; t < 4; t++) *(LAS u32x4*)(lds + kw[t]) = rb[s][t];
      PINM(); LOAD(s, rs); PINM();
      f32x4 sc = {0, 0, 0, 0};
#pragma unroll
      for (int t = 0; t < 4; t++) sc = mfma(*(LAS u32x4*)(lds + kr[t]), qf[t], sc);
      const unsigned u0 = cvtpk(sc[0], sc[1]), u1 = cvtpk(sc[2], sc[3]);
      z0 = (n & 7) == s ? u0 : z0; z1 = (n & 7) == s ? u1 : z1;
      PIN(z0); PIN(z1);
    }
#pragma unroll
    for (int j = 0; j < NTURN - 1; j++) { Z[j][0] = Z[j + 1][0]; Z[j][1] = Z[j + 1][1]; }
    Z[NTURN - 1][0] = z0; Z[NTURN - 1][1] = z1;
  }
  STAMP(15);
  unsigned sel[2][4];
  {
    float z[4 * NTURN];
#pragma unroll
    for (int j = 0; j < 2 * NTURN; j++) {
      const unsigned u = Z[j >> 1][j & 1], w = cvtpk(lo2f(u) * SCALE, hi2f(u) * SCALE);
      z[2 * j] = lo2f(w); z[2 * j + 1] = hi2f(w);
    }
    float mx = z[0];
#pragma unroll
    for (int j = 1; j < 4 * NTURN; j++) mx = fmaxf(mx, z[j]);
    mx = fmaxf(mx, dpp<XOR1>(mx)); mx = fmaxf(mx, dpp<XOR2>(mx)); mx = fmaxf(mx, dpp<HMIR>(mx));
    mx = x32_max(x16_max(mx));
    if (lane == 0 || lane == 8) *(LAS float*)(lds + L_ML + (n >> 3) * 32 + wave * 4) = mx;
#pragma unroll
    for (int k = 0; k < 4; k++) {
      const bool on = ((n & 7) >> 1) == k;
      sel[0][k] = on ? ((n & 1) ? 0x01000c0cu : 0x0c0c0100u) : 0x0c0c0c0cu;
      sel[1][k] = on ? ((n & 1) ? 0x03020c0cu : 0x0c0c0302u) : 0x0c0c0c0cu;
    }
    LDS_BARRIER();
    const f32x4 ma = *(LAS f32x4*)(lds + L_ML + (n >> 3) * 32), mb = *(LAS f32x4*)(lds + L_ML + (n >> 3) * 32 + 16);
    const float m2 = fmaxf(fmaxf(fmaxf(ma[0], ma[1]), fmaxf(ma[2], ma[3])), fmaxf(fmaxf(mb[0], mb[1]), fmaxf(mb[2], mb[3]))) * LOG2E;
    float l = 0.f;
#pragma unroll
    for (int j = 0; j < 2 * NTURN; j++) {
      const float ea = __builtin_amdgcn_exp2f(fmaf(z[2 * j], LOG2E, -m2)), eb = __builtin_amdgcn_exp2f(fmaf(z[2 * j + 1], LOG2E, -m2));
      l += ea + eb;
      Z[j >> 1][j & 1] = cvtpk(ea, eb);
      asm volatile("" : "+v"(Z[j >> 1][j & 1]));
    }
    l += dpp<XOR1>(l); l += dpp<XOR2>(l); l += dpp<HMIR>(l);
    l = x16_add(l, l); l = x32_add(l, l);
    if (lane == 0 || lane == 8) *(LAS float*)(lds + L_ML + 64 + (n >> 3) * 32 + wave * 4) = l;
    LDS_BARRIER();
    if (wave == 0) {
      const f32x4 la = *(LAS f32x4*)(lds + L_ML + 64 + (n >> 3) * 32), lb = *(LAS f32x4*)(lds + L_ML + 64 + (n >> 3) * 32 + 16);
      const float L = ((la[0] + la[1]) + (la[2] + la[3])) + ((lb[0] + lb[1]) + (lb[2] + lb[3]));
      if (lane == 0 || lane == 8)
        __builtin_amdgcn_raw_buffer_store_b128(u32x4{__float_as_uint(m2), tag, __float_as_uint(L), tag}, rws, (n >> 3) * 128, WS_ML + ((ab * 32 + 2 * kvh) * 8 + sp) * 16, AUX_SC1);
    }
  }
  const int rho = bid & 7, cg = bid >> 3;
  const int fb = bid >> 7, fi = bid & 127;
  const int e0 = (fi * 9) >> 1, e1 = (fi * 9 + 9) >> 1;
  auto ldwo = [&](int row0, int cnt) {
    const u32x4 ro = {(unsigned)(uintptr_t)p.wo, (unsigned)((uintptr_t)p.wo >> 32) & 0xffffu, 4096 * DM * 2, 0x00020000};
    const unsigned vo = (lane >> 2) * 1152 + (((lane & 3) ^ ((lane >> 4) & 2)) << 4);
#pragma unroll
    for (int t = 0; t < cnt; t++)
      asm volatile("s_mov_b32 m0, %0\n\ts_nop 0\n\tbuffer_load_dwordx4 %1, %2, %3 offen nt lds"
                   :: "s"((unsigned)(uintptr_t)lds + L_WO + (row0 + 16 * t) * 64), "v"(vo), "s"(ro), "s"((512 * rho + row0 + 16 * t) * 1152 + 64 * cg) : "memory");
  };
  PINM();
  u32x4 w1v, b1v, w2v; unsigned w1t, b1t, w2t, xh, b2v;
  auto ldw = [&]() {
    w1v = *(const u32x4*)(p.w1 + 8 * tid); b1v = *(const u32x4*)(p.b1 + 8 * tid); w2v = *(const u32x4*)(p.w2 + 8 * tid);
    w1t = p.w1[4096 + tid]; b1t = p.b1[4096 + tid]; w2t = p.w2[4096 + tid];
    xh = *(const unsigned*)(p.x + fb * DM + 2 * min(tid, 287));
    b2v = p.b2[0];
    PINM();
  };
  ldw();
  f32x4 acc = {0, 0, 0, 0};
  auto pv = [&](auto sc_, const unsigned (&P)[2], const u32x4 (&v)[4]) {
    constexpr int s = decltype(sc_)::value;
#pragma unroll
    for (int h = 0; h < 2; h++) {
      int b = __builtin_amdgcn_mov_dpp((int)P[h], 0x150 + s, 0xf, 0xf, true);
      b = __builtin_amdgcn_update_dpp(b, (int)P[h], 0x158 + s, 0xf, 0xc, false);
#pragma unroll
      for (int c = 0; c < 2; c++)
        acc = mfma(u32x4{__builtin_amdgcn_perm(b, b, sel[c][0]), __builtin_amdgcn_perm(b, b, sel[c][1]), __builtin_amdgcn_perm(b, b, sel[c][2]), __builtin_amdgcn_perm(b, b, sel[c][3])}, v[2 * h + c], acc);
    }
  };
  u32x4 gm[1];
  auto rml = __builtin_amdgcn_make_buffer_rsrc((void*)p.ws, 0, wave < 2 ? WS_END : 0, 0x00020000);
  auto ldm = [&](int) { return __builtin_amdgcn_raw_buffer_load_b128(rml, (lane & 7) * 16, WS_ML + (ab * 32 + 2 * kvh + wave) * 128, AUX_SC1); };
#define FOR8(X) X(0) X(1) X(2) X(3) X(4) X(5) X(6) X(7)
#pragma unroll 1
  for (int i = 0; i < NTURN - 1; i++) {
    so = sbase + sprd(i + 1, 10, 2);
    gm[0] = ldm(0);
#define VSTEP(s) if (s % BSTEP == 0) { SB(); __builtin_amdgcn_s_barrier(); SB(); } pv(IC<s>{}, Z[0], rb[s]); PIN(acc); LOAD(s, rv); SB();
    FOR8(VSTEP)
#pragma unroll
    for (int j = 0; j < NTURN - 1; j++) { Z[j][0] = Z[j + 1][0]; Z[j][1] = Z[j + 1][1]; }
  }
  STAMP(4);
  const unsigned hrow = (wave >> 2) * 32 + 4 * rho + (wave & 3);
  auto ld1 = [&](int s) { return __builtin_amdgcn_raw_buffer_load_b128(rws, lane * 16, WS_P1 + hrow * 8192 + s * 1024, AUX_SC1); };
  u32x4 go[8], bfr[16];
  float wgt = 0.f;
  {
    __builtin_amdgcn_s_barrier();
#define VLAST(s) pv(IC<s>{}, Z[0], rb[s]); SB();
    FOR8(VLAST)
    PIN(acc);
    if (wave < 2) {
      settle<1>(gm, tag, ldm);
      const float ms = __uint_as_float(gm[0][0]);
      float M = fmaxf(ms, dpp<XOR1>(ms)); M = fmaxf(M, dpp<XOR2>(M)); M = fmaxf(M, dpp<HMIR>(M));
      const float e = __builtin_amdgcn_exp2f(ms - M);
      float L = e * __uint_as_float(gm[0][2]); L += dpp<XOR1>(L); L += dpp<XOR2>(L); L += dpp<HMIR>(L);
      wgt = rdlane(e * __builtin_amdgcn_rcpf(L), sp);
    }
    PINM();
    if (wave < 4 && cg < 18) ldwo(128 * wave, 8);
    SB();
  }
  STAMP(5);
  *(LAS f32x4*)(lds + L_O + wave * 1024 + (g >> 1) * 512 + (8 * n + 4 * (g & 1)) * 4) = acc;
  LDS_BARRIER();
  STAMP(13);
  if (wave < 2) {
    f32x2 o[8];
#pragma unroll
    for (int w = 0; w < 8; w++) o[w] = *(LAS f32x2*)(lds + L_O + w * 1024 + wave * 512 + lane * 8);
    const f32x2 t = (((o[0] + o[1]) + (o[2] + o[3])) + ((o[4] + o[5]) + (o[6] + o[7]))) * wgt;
    __builtin_amdgcn_raw_buffer_store_b128(u32x4{__float_as_uint(t[0]), tag, __float_as_uint(t[1]), tag}, rws, lane * 16, WS_P1 + ((ab * 32 + 2 * kvh + wave) * 8 + sp) * 1024, AUX_SC1);
  }
  STAMP(6);
  if (cg < 18) {
#pragma unroll
    for (int s = 0; s < 8; s++) go[s] = ld1(s);
    unsigned olw = L_OL + (wave >> 2) * 1088 + (wave & 3) * 256 + lane * 4, olr = L_OL + (n & 1) * 1088 + 16 * g, p2o = (g * 8 * DM + n) * 8;
    PIN(olw); PIN(olr); PIN(p2o);
    VMWAIT(8);
    __builtin_amdgcn_s_barrier();
    PINM();
    if (wave < 2) {
#pragma unroll
      for (int s = 0; s < 16; s++) {
        const int a = L_WO + (32 * s + 8 * g + (n >> 2)) * 64 + ((wave ^ (g & 1)) << 5) + (n & 3) * 8;
        bfr[s] = read_tr(lds + a, lds + a + 256);
      }
    }
    settle<8>(go, tag, ld1);
    STAMP(7);
    const float o0 = sum8(go, 0), o1 = sum8(go, 2);
    *(LAS unsigned*)(lds + olw) = cvtpk(o0, o1);
    LDS_BARRIER();
    if (wave < 2) {
      u32x4 af[16];
#pragma unroll
      for (int s = 0; s < 16; s++) af[s] = *(LAS u32x4*)(lds + olr + 64 * s);
      SB();
      f32x4 d[4] = {};
#pragma unroll
      for (int s = 0; s < 16; s++) d[s & 3] = mfma(af[s], bfr[s], d[s & 3]);
      const float r = g ? (d[0][1] + d[1][1]) + (d[2][1] + d[3][1]) : (d[0][0] + d[1][0]) + (d[2][0] + d[3][0]);
      if (g < 2) __builtin_amdgcn_raw_buffer_store_b64(u32x2{__float_as_uint(r), tag}, rws, p2o, WS_P2 + (rho * DM + 32 * cg + 16 * wave) * 8, AUX_SC1);
    }
    STAMP(8);
  }
  PINM();
  constexpr float KS = 0.4686686930532063f;
  constexpr float BETA = 2.f * LOG2E * 0.7978845608028654f / KS;
  u32x4 gp[8];
  auto rp2 = __builtin_amdgcn_make_buffer_rsrc((void*)p.ws, 0, wave < 5 ? WS_END : 0, 0x00020000);
  auto ld2 = [&](int s) { return __builtin_amdgcn_raw_buffer_load_b128(rp2, min(tid, 287) * 16, WS_P2 + (fb * 8 + s) * DM * 8, AUX_SC1); };
#pragma unroll
  for (int s = 0; s < 8; s++) gp[s] = ld2(s);
  PINM();
  f32x2 W1[4], B1[4], W2[4];
#pragma unroll
  for (int j = 0; j < 4; j++) {
    W1[j] = f32x2{lo2f(w1v[j]), hi2f(w1v[j])} * KS;
    B1[j] = f32x2{lo2f(b1v[j]), hi2f(b1v[j])} * KS;
    W2[j] = f32x2{lo2f(w2v[j]), hi2f(w2v[j])} * (1.f / KS);
    PIN(W1[j]); PIN(B1[j]); PIN(W2[j]);
  }
  float W1t = lo2f(w1t) * KS, B1t = lo2f(b1t) * KS, W2t = lo2f(w2t) * (1.f / KS);
  PIN(W1t); PIN(B1t); PIN(W2t);
  if (wave < 5) {
    settle<8>(gp, tag, ld2);
    const unsigned at = cvtpk(sum8(gp, 0), sum8(gp, 2));
    const unsigned hh = cvtpk(lo2f(xh) + lo2f(at), hi2f(xh) + hi2f(at));
    if (tid < 288) *(LAS f32x2*)(lds + L_HS + tid * 8) = f32x2{lo2f(hh), hi2f(hh)};
  }
  STAMP(9);
  LDS_BARRIER();
  if (bid == 0 && tid == 0) __builtin_amdgcn_raw_buffer_store_b32(tag, rws, 0, WS_EPOCH, AUX_SC1);
  float hn[5];
  const float hown = *(LAS float*)(lds + L_HS + min(e0 + g + 4 * (lane >> 3 & 1), DM - 1) * 4);
  {
    float v[9];
#pragma unroll
    for (int i = 0; i < 9; i++) v[i] = *(LAS float*)(lds + L_HS + (lane + 64 * i) * 4);
#pragma unroll
    for (int e = 0; e < 5; e++) hn[e] = *(LAS float*)(lds + L_HS + min(e0 + e, DM - 1) * 4);
    const float mean = wave_sum(((v[0] + v[1]) + (v[2] + v[3])) + ((v[4] + v[5]) + (v[6] + v[7])) + v[8]) * (1.f / DM);
    float s2a = 0.f, s2b = 0.f;
#pragma unroll
    for (int i = 0; i < 9; i++) { const float a = v[i] - mean; if (i & 1) s2b = fmaf(a, a, s2b); else s2a = fmaf(a, a, s2a); }
    const float var = wave_sum(s2a + s2b) * (1.f / DM);
    const float rden = rden_of(var), mb = rbf(mean);
    const float mb2 = rbf((mean - mb) * rden), rden2 = rden_of(var * rden * rden);
#pragma unroll
    for (int e = 0; e < 5; e++) hn[e] = rbf(rbf(rbf(rbf(hn[e] - mb) * rden) - mb2) * rden2);
  }
  STAMP(10);
  auto term = [&](f32x2 h, f32x2 w1, f32x2 b1, f32x2 w2, f32x2 a) {
    const f32x2 v = __builtin_elementwise_fma(h, w1, b1);
    const f32x2 z = v * __builtin_elementwise_fma(v, v, f32x2{BETA, BETA});
    const f32x2 d = f32x2{__builtin_amdgcn_exp2f(-z[0]), __builtin_amdgcn_exp2f(-z[1])} + 1.f;
    return __builtin_elementwise_fma(v * w2, f32x2{__builtin_amdgcn_rcpf(d[0]), __builtin_amdgcn_rcpf(d[1])}, a);
  };
  float ya[5];
  {
    const f32x2 t01 = term(f32x2{hn[0], hn[1]}, f32x2{W1t, W1t}, f32x2{B1t, B1t}, f32x2{W2t, W2t}, f32x2{0.f, 0.f});
    const f32x2 t23 = term(f32x2{hn[2], hn[3]}, f32x2{W1t, W1t}, f32x2{B1t, B1t}, f32x2{W2t, W2t}, f32x2{0.f, 0.f});
    const float v4 = fmaf(hn[4], W1t, B1t);
    const float t4 = v4 * W2t * __builtin_amdgcn_rcpf(1.f + __builtin_amdgcn_exp2f(-(v4 * fmaf(v4, v4, BETA))));
#pragma unroll
    for (int e = 0; e < 5; e++) {
      f32x2 a = {e == 4 ? t4 : e < 2 ? t01[e & 1] : t23[e & 1], 0.f};
#pragma unroll
      for (int j = 0; j < 4; j++) a = term(f32x2{hn[e], hn[e]}, W1[j], B1[j], W2[j], a);
      ya[e] = a[0] + a[1];
    }
  }
  {
    const float r = row_sum(x16_add(x32_add(ya[0], ya[1]), x32_add(ya[2], ya[3])));
    const float r4 = wave_sum(ya[4]);
    if (n == 0) *(LAS float*)(lds + L_RED + ((g >> 1) + 2 * (g & 1)) * 32 + wave * 4) = r;
    if (lane == 1) *(LAS float*)(lds + L_RED + 4 * 32 + wave * 4) = r4;
  }
  STAMP(11);
  LDS_BARRIER();
  if (wave == 0) {
    const int e = g + 4 * (lane >> 3 & 1);
    const f32x4 a = *(LAS f32x4*)(lds + L_RED + min(e, 4) * 32), c = *(LAS f32x4*)(lds + L_RED + min(e, 4) * 32 + 16);
    const float y = rbf(rbf(((a[0] + a[1]) + (a[2] + a[3])) + ((c[0] + c[1]) + (c[2] + c[3]))) + lo2f(b2v));
    auto rout = __builtin_amdgcn_make_buffer_rsrc((void*)p.out, 0, 2 * DM * 2, 0x00020000);
    if ((lane & 7) == 0 && e0 + e < e1) __builtin_amdgcn_raw_buffer_store_b16((u16)cvtpk(hown + y, 0.f), rout, e * 2, (fb * DM + e0) * 2, AUX_SC1);
  }
  STAMP(12);
  asm volatile(".Lfused_last:");
}

extern "C" void kernel_launch(void* const* d_in, const int* in_sizes, int n_in, void* d_out, int out_size, void* d_ws, size_t ws_size, hipStream_t stream) {
  Params p;
  p.x = (const u16*)d_in[0]; p.K = (const char*)d_in[1]; p.V = (const char*)d_in[2];
  p.wq = (const char*)d_in[3]; p.wo = (const char*)d_in[4];
  p.w1 = (const u16*)d_in[6]; p.b1 = (const u16*)d_in[7]; p.w2 = (const u16*)d_in[8]; p.b2 = (const u16*)d_in[9];
  p.out = (u16*)d_out; p.ws = (char*)d_ws;
  fused_kernel<<<256, 512, 0, stream>>>(p);
}
```

```cpp
#include <hip/hip_runtime.h>
#include <hip/hip_bf16.h>
#include <stdint.h>

#define DIAG 0
#define TOUCH0 1
#define RPERM 0
#define SLEEP 1
#define WOPOS 1
#define NPT 3
#define PLACE 0
#define XT 0
#define WLATE 0
#define FIN0 254
#define BSTEP 8
#define PERM 21,16,9,10,8,20,17,18,14,19,13,15,12,22,11
#define BAR0 1

typedef unsigned u32x4 __attribute__((ext_vector_type(4)));
typedef unsigned u32x3 __attribute__((ext_vector_type(3)));
typedef unsigned u32x2 __attribute__((ext_vector_type(2)));
typedef float f32x4 __attribute__((ext_vector_type(4)));
typedef float f32x2 __attribute__((ext_vector_type(2)));
typedef __bf16 bf16x8 __attribute__((ext_vector_type(8)));
typedef __bf16 bf16x2 __attribute__((ext_vector_type(2)));
typedef short s16x4 __attribute__((ext_vector_type(4)));
typedef unsigned short u16;
#define LAS __attribute__((address_space(3)))

constexpr int DM = 576, FF = 4608;
constexpr float LOG2E = 1.4426950408889634f;
constexpr float SCALE = 0.08838834764831845f;
constexpr int NSL = 8;
constexpr int NTURN = 32 / NSL;
constexpr int NP = 8 * NPT;
constexpr int NTAB = 256 * NP;
constexpr unsigned TAB0 = (NPT == 3 ? 127 - 19 : 127 - 13) << 7;
constexpr unsigned SPIN_CAP = 1u << 16;
constexpr int AUX_NT = 2, AUX_SC1 = 16;

constexpr unsigned WS_EPOCH = 0;
constexpr unsigned WS_QG = 4096;
constexpr unsigned WS_P1 = WS_QG + 32 * 8 * 2048;
constexpr unsigned WS_ML = WS_P1 + 512 * 1024;
constexpr unsigned WS_P2 = WS_ML + 512 * 16;
constexpr unsigned WS_T = WS_P2 + 16 * DM * 8;
constexpr unsigned WS_END = WS_T + NTAB * 8;
constexpr unsigned WS_STAMP = 2u << 20;

constexpr int L_Q = 0;
constexpr int L_ML = 512;
constexpr int L_O = 640;
constexpr int L_OL = L_O + 8192;
constexpr int L_HS = L_OL + 2 * 1088;
constexpr int L_RED = L_HS + DM * 4;
constexpr int L_WO = L_RED + NP * 32;
constexpr int L_TAB = L_WO + 32768;
constexpr int L_END = L_TAB + NTAB * 8;

struct Params {
  const u16* x; const char* K; const char* V; const char* wq; const char* wo;
  const u16* w1; const u16* b1; const u16* w2; const u16* b2; u16* out; char* ws;
};

__device__ __forceinline__ float lo2f(unsigned v) { return __uint_as_float(v << 16); }
__device__ __forceinline__ float hi2f(unsigned v) { return __uint_as_float(v & 0xffff0000u); }
__device__ __forceinline__ unsigned cvtpk(float lo, float hi) {
  f32x2 v = {lo, hi};
  return __builtin_bit_cast(unsigned, __builtin_convertvector(v, bf16x2));
}
__device__ __forceinline__ float rbf(float x) { return lo2f(cvtpk(x, x)); }
template <int CTRL> __device__ __forceinline__ float dpp(float x) {
  return __builtin_bit_cast(float, __builtin_amdgcn_mov_dpp(__builtin_bit_cast(int, x), CTRL, 0xf, 0xf, true));
}
constexpr int XOR1 = 0xB1, XOR2 = 0x4E, HMIR = 0x141, MIR = 0x140;
__device__ __forceinline__ float row_sum(float x) {
  x += dpp<XOR1>(x); x += dpp<XOR2>(x); x += dpp<HMIR>(x); x += dpp<MIR>(x); return x;
}
__device__ __forceinline__ float x16_add(float a, float b) {
  auto s = __builtin_amdgcn_permlane16_swap(__float_as_uint(a), __float_as_uint(b), false, false);
  return __uint_as_float(s[0]) + __uint_as_float(s[1]);
}
__device__ __forceinline__ float x32_add(float a, float b) {
  auto s = __builtin_amdgcn_permlane32_swap(__float_as_uint(a), __float_as_uint(b), false, false);
  return __uint_as_float(s[0]) + __uint_as_float(s[1]);
}
__device__ __forceinline__ float x16_max(float x) {
  auto s = __builtin_amdgcn_permlane16_swap(__float_as_uint(x), __float_as_uint(x), false, false);
  return fmaxf(__uint_as_float(s[0]), __uint_as_float(s[1]));
}
__device__ __forceinline__ float x32_max(float x) {
  auto s = __builtin_amdgcn_permlane32_swap(__float_as_uint(x), __float_as_uint(x), false, false);
  return fmaxf(__uint_as_float(s[0]), __uint_as_float(s[1]));
}
__device__ __forceinline__ float wave_sum(float x) { x = row_sum(x); x = x16_add(x, x); return x32_add(x, x); }
__device__ __forceinline__ float rdlane(float x, int l) { return __builtin_bit_cast(float, __builtin_amdgcn_readlane(__builtin_bit_cast(int, x), l)); }
__device__ __forceinline__ float rden_of(float var) {
  return __builtin_amdgcn_rcpf(rbf(__builtin_amdgcn_sqrtf(rbf(rbf(var) + 1e-5f))));
}
__device__ __forceinline__ f32x4 mfma(u32x4 a, u32x4 b, f32x4 c) {
  return __builtin_amdgcn_mfma_f32_16x16x32_bf16(__builtin_bit_cast(bf16x8, a), __builtin_bit_cast(bf16x8, b), c, 0, 0, 0);
}
__device__ __forceinline__ u32x4 read_tr(LAS char* a, LAS char* b) {
  const u32x2 ua = __builtin_bit_cast(u32x2, __builtin_amdgcn_ds_read_tr16_b64_v4i16((LAS s16x4*)a));
  const u32x2 ub = __builtin_bit_cast(u32x2, __builtin_amdgcn_ds_read_tr16_b64_v4i16((LAS s16x4*)b));
  return u32x4{ua[0], ua[1], ub[0], ub[1]};
}
#define SB() __builtin_amdgcn_sched_barrier(0)
#define PIN(x) do { asm volatile("" : "+v"(x) :: "memory"); __builtin_amdgcn_sched_barrier(0); } while (0)
#define PINM() do { asm volatile("" ::: "memory"); __builtin_amdgcn_sched_barrier(0); } while (0)
#define SINK(x) asm volatile("" :: "v"(x))
#define VMWAIT(n) do { __builtin_amdgcn_sched_barrier(0); asm volatile("s_waitcnt vmcnt(" #n ")" ::: "memory"); __builtin_amdgcn_sched_barrier(0); } while (0)
#define LDS_BARRIER() do { asm volatile("s_waitcnt lgkmcnt(0)" ::: "memory"); __builtin_amdgcn_s_barrier(); asm volatile("" ::: "memory"); } while (0)
#define STAMP(i) do {} while (0)

constexpr float KS = 0.4686686930532063f;
constexpr float BETA = 2.f * LOG2E * 0.7978845608028654f / KS;
__device__ __forceinline__ f32x2 term(f32x2 h, f32x2 w1, f32x2 b1, f32x2 w2, f32x2 a) {
  const f32x2 v = __builtin_elementwise_fma(h, w1, b1);
  const f32x2 z = v * __builtin_elementwise_fma(v, v, f32x2{BETA, BETA});
  const f32x2 d = f32x2{__builtin_amdgcn_exp2f(-z[0]), __builtin_amdgcn_exp2f(-z[1])} + 1.f;
  return __builtin_elementwise_fma(v * w2, f32x2{__builtin_amdgcn_rcpf(d[0]), __builtin_amdgcn_rcpf(d[1])}, a);
}
template <int V> struct IC { static constexpr int value = V; };
template <int N, class F>
__device__ __forceinline__ void settle(u32x4 (&g)[N], unsigned tag, F load) {
#pragma unroll 1
  for (unsigned spin = 0; spin < SPIN_CAP; spin++) {
    unsigned y[N], any = 0;
#pragma unroll
    for (int s = 0; s < N; s++) { y[s] = __builtin_amdgcn_bitop3_b32(g[s][1], g[s][3], tag, 0x7e); any |= y[s]; }
    if (__builtin_amdgcn_ballot_w64(any != 0) == 0) break;
    __builtin_amdgcn_s_sleep(SLEEP);
    asm volatile("" ::: "memory");
#pragma unroll
    for (int s = 0; s < N; s++)
      if (__builtin_amdgcn_ballot_w64(y[s] != 0) != 0) g[s] = load(s);
  }
}
__device__ __forceinline__ float fadd(float a, float b) { float r; asm("v_add_f32 %0, %1, %2" : "=v"(r) : "v"(a), "v"(b)); return r; }
__device__ __forceinline__ float sum8(const u32x4 (&g)[8], int c) {
  float t[4];
#pragma unroll
  for (int i = 0; i < 4; i++) t[i] = fadd(__uint_as_float(g[2 * i][c]), __uint_as_float(g[2 * i + 1][c]));
  return fadd(fadd(t[0], t[1]), fadd(t[2], t[3]));
}

__global__ void __launch_bounds__(512, 2) fused_kernel(Params p) {
  __shared__ __attribute__((aligned(16))) char lds_[L_END];
  LAS char* const lds = (LAS char*)lds_;
  const int tid = threadIdx.x, lane = tid & 63;
  const int wave = __builtin_amdgcn_readfirstlane(tid >> 6);
  const int bid = blockIdx.x;
  const int g = lane >> 4, n = lane & 15;
  STAMP(0);
  auto rws = __builtin_amdgcn_make_buffer_rsrc((void*)p.ws, 0, WS_END, 0x00020000);
  auto rk = __builtin_amdgcn_make_buffer_rsrc((void*)p.K, 0, 256u << 20, 0x00020000);
  auto rv = __builtin_amdgcn_make_buffer_rsrc((void*)p.V, 0, 256u << 20, 0x00020000);
  char* pc; unsigned pclen;
  asm volatile("s_getpc_b64 %0\n.Lfused_first:\n\ts_mov_b32 %1, .Lfused_last-.Lfused_first" : "=s"(pc), "=s"(pclen));
  auto rc = __builtin_amdgcn_make_buffer_rsrc((void*)pc, 0, pclen, 0x00020000);
  unsigned epoch = *(const __attribute__((address_space(4))) unsigned*)(p.ws + WS_EPOCH);

  const int ab = bid >> 7, kvh = (bid >> 3) & 15, sp = bid & 7;
  constexpr int PB[15] = {PERM};
  auto sprd = [&](unsigned v, int first, int nb) { unsigned r = 0; for (int i = 0; i < nb; i++) r |= ((v >> i) & 1u) << PB[first + i]; return r; };
  unsigned va[4];
#pragma unroll
  for (int t = 0; t < 4; t++) va[t] = sprd(g + 4 * t, 0, 4) + n * 16;
  const unsigned sbase = ((unsigned)(bid >> 3) << 23) + sprd(wave, 4, 3);
  auto turn_off = [&](int i) { return sbase + sprd(i, 10, 2) + sprd(sp ^ (XT >> (4 * (i & 3))), 12, 3); };
  unsigned so = turn_off(0);
#define SO(s) (so + sprd(s, 7, 3))
  u32x4 rb[NSL][4];
#define LOAD(s, rs) _Pragma("unroll") for (int t = 0; t < 4; t++) rb[s][t] = __builtin_amdgcn_raw_buffer_load_b128(rs, va[t], SO(s), AUX_NT)

  unsigned msk[4];
  auto mkmask = [&]() {
    int nn = n;
    asm volatile("" : "+v"(nn));
#pragma unroll
    for (int k = 0; k < 4; k++) msk[k] = (((nn & 7) >> 1) == k) ? ((nn & 1) ? 0xffff0000u : 0x0000ffffu) : 0u;
  };

  unsigned touch = 0;
  touch = __builtin_amdgcn_raw_buffer_load_b32(rc, lane * 64, ((wave + 7) & 7) * 4096, 0);
  PINM();
  if (wave == 0) {
    const int hq = bid >> 3, ks = bid & 7;
    auto rx = __builtin_amdgcn_make_buffer_rsrc((void*)p.x, 0, 2 * DM * 2, 0x00020000);
    auto rq = __builtin_amdgcn_make_buffer_rsrc((void*)p.wq, 0, DM * 4096 * 2, 0x00020000);
    u32x3 xs[3], xm[3];
    u32x4 wb[18];
#pragma unroll
    for (int i = 0; i < 3; i++) xs[i] = __builtin_amdgcn_raw_buffer_load_b96(rx, lane * 36 + 12 * i, 0, 0);
#pragma unroll
    for (int i = 0; i < 3; i++) xm[i] = __builtin_amdgcn_raw_buffer_load_b96(rx, ((n >> 3) * 32 + 4 * ks + g) * 36 + 12 * i, 0, 0);
#pragma unroll
    for (int t = 0; t < 18; t++) wb[t] = __builtin_amdgcn_raw_buffer_load_b128(rq, g * 18 * 8192 + n * 16, (72 * ks + t) * 8192 + hq * 256, AUX_NT);
    PINM();
    asm volatile("s_barrier" ::: "memory");
    PINM();
    mkmask();
    float s1 = 0.f;
#pragma unroll
    for (int i = 0; i < 9; i++) { const unsigned u = xs[i / 3][i % 3]; s1 += lo2f(u) + hi2f(u); }
    s1 = row_sum(s1);
    const float mean = x16_add(s1, s1) * (1.f / DM);
    float s2 = 0.f;
#pragma unroll
    for (int i = 0; i < 9; i++) { const unsigned u = xs[i / 3][i % 3]; const float a = lo2f(u) - mean, c = hi2f(u) - mean; s2 += a * a + c * c; }
    s2 = row_sum(s2);
    const float rden = rden_of(x16_add(s2, s2) * (1.f / DM)), mb = rbf(mean);
    const float mb0 = rdlane(mb, 0), mb1 = rdlane(mb, 32), rd0 = rdlane(rden, 0), rd1 = rdlane(rden, 32);
    const float mb_ = (n < 8) ? mb0 : mb1, rd_ = (n < 8) ? rd0 : rd1;
    f32x4 qa = {0, 0, 0, 0};
#pragma unroll
    for (int i = 0; i < 9; i++) {
      const unsigned u = xm[i / 3][i % 3], d = cvtpk(lo2f(u) - mb_, hi2f(u) - mb_);
      const float y0 = lo2f(d) * rd_, y1 = hi2f(d) * rd_;
      const unsigned p0 = cvtpk(y0, y0), p1 = cvtpk(y1, y1);
      qa = mfma(u32x4{p0 & msk[0], p0 & msk[1], p0 & msk[2], p0 & msk[3]}, wb[2 * i], qa);
      qa = mfma(u32x4{p1 & msk[0], p1 & msk[1], p1 & msk[2], p1 & msk[3]}, wb[2 * i + 1], qa);
    }
    unsigned tag = epoch;
    asm volatile("" : "+s"(tag));
    tag += 1;
    __builtin_amdgcn_raw_buffer_store_b128(u32x4{__float_as_uint(qa[0]), tag, __float_as_uint(qa[1]), tag}, rws, lane * 16, WS_QG + bid * 2048, AUX_SC1);
    __builtin_amdgcn_raw_buffer_store_b128(u32x4{__float_as_uint(qa[2]), tag, __float_as_uint(qa[3]), tag}, rws, lane * 16, WS_QG + bid * 2048 + 1024, AUX_SC1);
    STAMP(1);
  }
  else asm volatile("s_barrier" ::: "memory");
  PINM();
#pragma unroll
  for (int t = 0; t < 4; t++) PIN(va[t]);
#pragma unroll
  for (int s = 0; s < NSL; s++) { LOAD(s, rk); PINM(); }
  asm volatile("" : "+s"(epoch));
  const unsigned tag = epoch + 1;
  const bool fin = bid >= FIN0 && bid < FIN0 + 2;
  const int fb = bid & 1;
  u32x4 w1v, b1v, w2v; unsigned w1t, b1t, w2t, xh, b2v;
  auto ldw = [&]() {
    w1v = *(const u32x4*)(p.w1 + 8 * tid); b1v = *(const u32x4*)(p.b1 + 8 * tid); w2v = *(const u32x4*)(p.w2 + 8 * tid);
    w1t = p.w1[4096 + tid]; b1t = p.b1[4096 + tid]; w2t = p.w2[4096 + tid];
    xh = *(const unsigned*)(p.x + fb * DM + 2 * min(tid, 287)); b2v = p.b2[0];
    PINM();
  };
  if (!WLATE) ldw();
  unsigned kw[4], kr[4];
#pragma unroll
  for (int t = 0; t < 4; t++) {
    kw[t] = L_WO + wave * 4096 + (4 * g + t) * 256 + ((n ^ (4 * g + t)) << 4);
    kr[t] = L_WO + wave * 4096 + n * 256 + (((4 * t + g) ^ n) << 4);
    PIN(kw[t]); PIN(kr[t]);
  }
  PINM();
  if (wave == 1 || wave == 2) {
    u32x4 pq[8];
    auto ldq = [&](int s) {
      return __builtin_amdgcn_raw_buffer_load_b128(rws, (lane >> 5) * 1024 + (32 * ab + (lane & 31)) * 16, WS_QG + ((2 * kvh + wave - 1) * 8 + s) * 2048, AUX_SC1);
    };
#pragma unroll
    for (int s = 0; s < 8; s++) pq[s] = ldq(s);
    STAMP(14);
    settle<8>(pq, tag, ldq);
    const float qa = sum8(pq, 0), qc = sum8(pq, 2);
    *(LAS unsigned*)(lds + L_Q + (wave - 1) * 256 + (8 * n + 4 * (g & 1) + 2 * (lane >> 5)) * 2) = cvtpk(qa, qc);
    STAMP(2);
  }
  SINK(touch);
  LDS_BARRIER();
  u32x4 qf[4];
#pragma unroll
  for (int s = 0; s < 4; s++) qf[s] = *(LAS u32x4*)(lds + L_Q + (n >> 3) * 256 + (32 * s + 8 * g) * 2);
  STAMP(3);

  unsigned Z[NTURN][2];
  f32x2 W1[4], B1[4], W2[4];
  float W1t, B1t, W2t, ya[4];
  auto point2 = [&](int k, unsigned t) {
    const unsigned hb = t >= NTAB / 2 ? t - NTAB / 2 + TAB0 + 0x8000u : t + TAB0;
    const float h0 = lo2f(hb), h1 = lo2f(hb + 1);
    const f32x2 tt = term(f32x2{h0, h1}, f32x2{W1t, W1t}, f32x2{B1t, B1t}, f32x2{W2t, W2t}, f32x2{0.f, 0.f});
    f32x2 a = {tt[0], 0.f}, c = {tt[1], 0.f};
#pragma unroll
    for (int j = 0; j < 4; j++) { a = term(f32x2{h0, h0}, W1[j], B1[j], W2[j], a); c = term(f32x2{h1, h1}, W1[j], B1[j], W2[j], c); }
    ya[k] = a[0] + a[1]; ya[k + 1] = c[0] + c[1];
    PIN(ya[k]); PIN(ya[k + 1]);
  };
  constexpr int PT = WLATE ? NP / 2 : 8, T0 = WLATE ? 2 : 1;
  auto kturn = [&](auto tab_, auto rs, int i) {
    constexpr bool tab = decltype(tab_)::value;
    const bool on = WLATE || NPT == NTURN - 1 || i <= NPT;
    unsigned z0 = 0, z1 = 0;
#pragma unroll
    for (int s = 0; s < NSL; s++) {
      if (s % BSTEP == 0) { SB(); __builtin_amdgcn_s_barrier(); SB(); }
#pragma unroll
      for (int t = 0; t < 4; t++) *(LAS u32x4*)(lds + kw[t]) = rb[s][t];
      PINM(); LOAD(s, rs); PINM();
      f32x4 sc = {0, 0, 0, 0};
#pragma unroll
      for (int t = 0; t < 4; t++) sc = mfma(*(LAS u32x4*)(lds + kr[t]), qf[t], sc);
      const unsigned u0 = cvtpk(sc[0], sc[1]), u1 = cvtpk(sc[2], sc[3]);
      z0 = (n & 7) == s ? u0 : z0; z1 = (n & 7) == s ? u1 : z1;
      PIN(z0); PIN(z1);
      if (tab && on) {
        const int k0 = (PLACE == 1 ? 0 : s * PT / 16) * 2, k1 = (PLACE == 1 ? (s == 7) * PT / 2 : (s + 1) * PT / 16) * 2;
#pragma unroll
        for (int k = k0; k < k1; k += 2) {
          point2(k & 3, bid * NP + PT * (i - T0) + k);
          if (k & 2) {
            const float r = row_sum(x16_add(x32_add(ya[0], ya[1]), x32_add(ya[2], ya[3])));
            if (n == 0) *(LAS float*)(lds + L_RED + (PT * (i - T0) + (k & ~3) + (g >> 1) + 2 * (g & 1)) * 32 + wave * 4) = r;
            PINM();
          }
        }
      }
    }
#pragma unroll
    for (int j = 0; j < NTURN - 1; j++) { Z[j][0] = Z[j + 1][0]; Z[j][1] = Z[j + 1][1]; }
    Z[NTURN - 1][0] = z0; Z[NTURN - 1][1] = z1;
  };
#pragma unroll 1
  for (int i = 0; i < T0; i++) {
    so = turn_off(i + 1);
    if (WLATE && i == T0 - 1) ldw();
    kturn(IC<0>{}, rk, i);
  }
#pragma unroll
  for (int j = 0; j < 4; j++) {
    W1[j] = f32x2{lo2f(w1v[j]), hi2f(w1v[j])} * KS;
    B1[j] = f32x2{lo2f(b1v[j]), hi2f(b1v[j])} * KS;
    W2[j] = f32x2{lo2f(w2v[j]), hi2f(w2v[j])} * (1.f / KS);
    PIN(W1[j]); PIN(B1[j]); PIN(W2[j]);
  }
  W1t = lo2f(w1t) * KS; B1t = lo2f(b1t) * KS; W2t = lo2f(w2t) * (1.f / KS);
  PIN(W1t); PIN(B1t); PIN(W2t);
#pragma unroll 1
  for (int i = T0; i < NTURN; i++) {
    const bool last = i == NTURN - 1;
    so = turn_off(i + 1);
    kturn(IC<1>{}, __builtin_amdgcn_make_buffer_rsrc((void*)(last ? p.V : p.K), 0, 256u << 20, 0x00020000), i);
  }
  STAMP(15);
  unsigned sel[2][4];
  {
    float z[4 * NTURN];
#pragma unroll
    for (int j = 0; j < 2 * NTURN; j++) {
      const unsigned u = Z[j >> 1][j & 1], w = cvtpk(lo2f(u) * SCALE, hi2f(u) * SCALE);
      z[2 * j] = lo2f(w); z[2 * j + 1] = hi2f(w);
    }
    float mx = z[0];
#pragma unroll
    for (int j = 1; j < 4 * NTURN; j++) mx = fmaxf(mx, z[j]);
    mx = fmaxf(mx, dpp<XOR1>(mx)); mx = fmaxf(mx, dpp<XOR2>(mx)); mx = fmaxf(mx, dpp<HMIR>(mx));
    mx = x32_max(x16_max(mx));
    if (lane == 0 || lane == 8) *(LAS float*)(lds + L_ML + (n >> 3) * 32 + wave * 4) = mx;
#pragma unroll
    for (int k = 0; k < 4; k++) {
      const bool on = ((n & 7) >> 1) == k;
      sel[0][k] = on ? ((n & 1) ? 0x01000c0cu : 0x0c0c0100u) : 0x0c0c0c0cu;
      sel[1][k] = on ? ((n & 1) ? 0x03020c0cu : 0x0c0c0302u) : 0x0c0c0c0cu;
    }
    LDS_BARRIER();
    if (wave == 1 && lane < NP) {
      const f32x4 a = *(LAS f32x4*)(lds + L_RED + lane * 32), c = *(LAS f32x4*)(lds + L_RED + lane * 32 + 16);
      __builtin_amdgcn_raw_buffer_store_b64(u32x2{__float_as_uint(((a[0] + a[1]) + (a[2] + a[3])) + ((c[0] + c[1]) + (c[2] + c[3]))), tag}, rws, lane * 8, WS_T + bid * NP * 8, AUX_SC1);
    }
    PINM();
    const f32x4 ma = *(LAS f32x4*)(lds + L_ML + (n >> 3) * 32), mb = *(LAS f32x4*)(lds + L_ML + (n >> 3) * 32 + 16);
    const float m2 = fmaxf(fmaxf(fmaxf(ma[0], ma[1]), fmaxf(ma[2], ma[3])), fmaxf(fmaxf(mb[0], mb[1]), fmaxf(mb[2], mb[3]))) * LOG2E;
    float l = 0.f;
#pragma unroll
    for (int j = 0; j < 2 * NTURN; j++) {
      const float ea = __builtin_amdgcn_exp2f(fmaf(z[2 * j], LOG2E, -m2)), eb = __builtin_amdgcn_exp2f(fmaf(z[2 * j + 1], LOG2E, -m2));
      l += ea + eb;
      Z[j >> 1][j & 1] = cvtpk(ea, eb);
      asm volatile("" : "+v"(Z[j >> 1][j & 1]));
    }
    l += dpp<XOR1>(l); l += dpp<XOR2>(l); l += dpp<HMIR>(l);
    l = x16_add(l, l); l = x32_add(l, l);
    if (lane == 0 || lane == 8) *(LAS float*)(lds + L_ML + 64 + (n >> 3) * 32 + wave * 4) = l;
    LDS_BARRIER();
    if (wave == 0) {
      const f32x4 la = *(LAS f32x4*)(lds + L_ML + 64 + (n >> 3) * 32), lb = *(LAS f32x4*)(lds + L_ML + 64 + (n >> 3) * 32 + 16);
      const float L = ((la[0] + la[1]) + (la[2] + la[3])) + ((lb[0] + lb[1]) + (lb[2] + lb[3]));
      if (lane == 0 || lane == 8)
        __builtin_amdgcn_raw_buffer_store_b128(u32x4{__float_as_uint(m2), tag, __float_as_uint(L), tag}, rws, (n >> 3) * 128, WS_ML + ((ab * 32 + 2 * kvh) * 8 + sp) * 16, AUX_SC1);
    }
  }
  const int rho = bid & 7, cg = bid >> 3;
  auto ldwo = [&](int row0, int cnt) {
    const u32x4 ro = {(unsigned)(uintptr_t)p.wo, (unsigned)((uintptr_t)p.wo >> 32) & 0xffffu, 4096 * DM * 2, 0x00020000};
    const unsigned vo = (lane >> 2) * 1152 + (((lane & 3) ^ ((lane >> 4) & 2)) << 4);
#pragma unroll
    for (int t = 0; t < cnt; t++)
      asm volatile("s_mov_b32 m0, %0\n\ts_nop 0\n\tbuffer_load_dwordx4 %1, %2, %3 offen nt lds"
                   :: "s"((unsigned)(uintptr_t)lds + L_WO + (row0 + 16 * t) * 64), "v"(vo), "s"(ro), "s"((512 * rho + row0 + 16 * t) * 1152 + 64 * cg) : "memory");
  };
  PINM();
  f32x4 acc = {0, 0, 0, 0};
  auto pv = [&](auto sc_, const unsigned (&P)[2], const u32x4 (&v)[4]) {
    constexpr int s = decltype(sc_)::value;
#pragma unroll
    for (int h = 0; h < 2; h++) {
      int b = __builtin_amdgcn_mov_dpp((int)P[h], 0x150 + s, 0xf, 0xf, true);
      b = __builtin_amdgcn_update_dpp(b, (int)P[h], 0x158 + s, 0xf, 0xc, false);
#pragma unroll
      for (int c = 0; c < 2; c++)
        acc = mfma(u32x4{__builtin_amdgcn_perm(b, b, sel[c][0]), __builtin_amdgcn_perm(b, b, sel[c][1]), __builtin_amdgcn_perm(b, b, sel[c][2]), __builtin_amdgcn_perm(b, b, sel[c][3])}, v[2 * h + c], acc);
    }
  };
  u32x4 gm[1];
  auto rml = __builtin_amdgcn_make_buffer_rsrc((void*)p.ws, 0, wave < 2 ? WS_END : 0, 0x00020000);
  auto ldm = [&](int) { return __builtin_amdgcn_raw_buffer_load_b128(rml, (lane & 7) * 16, WS_ML + (ab * 32 + 2 * kvh + wave) * 128, AUX_SC1); };
#define FOR8(X) X(0) X(1) X(2) X(3) X(4) X(5) X(6) X(7)
#pragma unroll 1
  for (int i = 0; i < NTURN - 1; i++) {
    so = turn_off(i + 1);
    gm[0] = ldm(0);
    if (fin && i == NTURN - 2) {
      const u32x4 rt = {(unsigned)(uintptr_t)p.ws, (unsigned)((uintptr_t)p.ws >> 32) & 0xffffu, WS_END, 0x00020000};
#pragma unroll
      for (int t = 0; t < NP / 4; t++)
        asm volatile("s_mov_b32 m0, %0\n\ts_nop 0\n\tbuffer_load_dwordx4 %1, %2, %3 offen sc1 lds"
                     :: "s"((unsigned)(uintptr_t)lds + L_TAB + (wave * (NP / 4) + t) * 1024), "v"(lane * 16), "s"(rt), "s"(WS_T + (wave * (NP / 4) + t) * 1024) : "memory");
    }
    PINM();
#define VSTEP(s) if (s % BSTEP == 0) { SB(); __builtin_amdgcn_s_barrier(); SB(); } pv(IC<s>{}, Z[0], rb[s]); PIN(acc); LOAD(s, rv); SB();
    FOR8(VSTEP)
#pragma unroll
    for (int j = 0; j < NTURN - 1; j++) { Z[j][0] = Z[j + 1][0]; Z[j][1] = Z[j + 1][1]; }
  }
  STAMP(4);
  const unsigned hrow = (wave >> 2) * 32 + 4 * rho + (wave & 3);
  auto ld1 = [&](int s) { return __builtin_amdgcn_raw_buffer_load_b128(rws, lane * 16, WS_P1 + hrow * 8192 + s * 1024, AUX_SC1); };
  u32x4 go[8], bfr[16];
  float wgt = 0.f;
  {
    __builtin_amdgcn_s_barrier();
#define VLAST(s) pv(IC<s>{}, Z[0], rb[s]); SB();
    FOR8(VLAST)
    PIN(acc);
    if (wave < 2) {
      settle<1>(gm, tag, ldm);
      const float ms = __uint_as_float(gm[0][0]);
      float M = fmaxf(ms, dpp<XOR1>(ms)); M = fmaxf(M, dpp<XOR2>(M)); M = fmaxf(M, dpp<HMIR>(M));
      const float e = __builtin_amdgcn_exp2f(ms - M);
      float L = e * __uint_as_float(gm[0][2]); L += dpp<XOR1>(L); L += dpp<XOR2>(L); L += dpp<HMIR>(L);
      wgt = rdlane(e * __builtin_amdgcn_rcpf(L), sp);
    }
    PINM();
    if (wave < 4 && cg < 18) ldwo(128 * wave, 8);
    SB();
  }
  STAMP(5);
  *(LAS f32x4*)(lds + L_O + wave * 1024 + (g >> 1) * 512 + (8 * n + 4 * (g & 1)) * 4) = acc;
  LDS_BARRIER();
  STAMP(13);
  if (wave < 2) {
    f32x2 o[8];
#pragma unroll
    for (int w = 0; w < 8; w++) o[w] = *(LAS f32x2*)(lds + L_O + w * 1024 + wave * 512 + lane * 8);
    const f32x2 t = (((o[0] + o[1]) + (o[2] + o[3])) + ((o[4] + o[5]) + (o[6] + o[7]))) * wgt;
    __builtin_amdgcn_raw_buffer_store_b128(u32x4{__float_as_uint(t[0]), tag, __float_as_uint(t[1]), tag}, rws, lane * 16, WS_P1 + ((ab * 32 + 2 * kvh + wave) * 8 + sp) * 1024, AUX_SC1);
  }
  STAMP(6);
  if (cg < 18) {
#pragma unroll
    for (int s = 0; s < 8; s++) go[s] = ld1(s);
    unsigned olw = L_OL + (wave >> 2) * 1088 + (wave & 3) * 256 + lane * 4, olr = L_OL + (n & 1) * 1088 + 16 * g, p2o = (g * 8 * DM + n) * 8;
    PIN(olw); PIN(olr); PIN(p2o);
    VMWAIT(8);
    __builtin_amdgcn_s_barrier();
    PINM();
    if (wave < 2) {
#pragma unroll
      for (int s = 0; s < 16; s++) {
        const int a = L_WO + (32 * s + 8 * g + (n >> 2)) * 64 + ((wave ^ (g & 1)) << 5) + (n & 3) * 8;
        bfr[s] = read_tr(lds + a, lds + a + 256);
      }
    }
    settle<8>(go, tag, ld1);
    STAMP(7);
    const float o0 = sum8(go, 0), o1 = sum8(go, 2);
    *(LAS unsigned*)(lds + olw) = cvtpk(o0, o1);
    LDS_BARRIER();
    if (wave < 2) {
      u32x4 af[16];
#pragma unroll
      for (int s = 0; s < 16; s++) af[s] = *(LAS u32x4*)(lds + olr + 64 * s);
      SB();
      f32x4 d[4] = {};
#pragma unroll
      for (int s = 0; s < 16; s++) d[s & 3] = mfma(af[s], bfr[s], d[s & 3]);
      const float r = g ? (d[0][1] + d[1][1]) + (d[2][1] + d[3][1]) : (d[0][0] + d[1][0]) + (d[2][0] + d[3][0]);
      if (g < 2) __builtin_amdgcn_raw_buffer_store_b64(u32x2{__float_as_uint(r), tag}, rws, p2o, WS_P2 + (rho * DM + 32 * cg + 16 * wave) * 8, AUX_SC1);
    }
    STAMP(8);
  }
  PINM();
  if (fin) {
    unsigned hh = 0;
    if (wave < 5) {
      u32x4 gp[8];
      auto ld2 = [&](int s) { return __builtin_amdgcn_raw_buffer_load_b128(rws, min(tid, 287) * 16, WS_P2 + (fb * 8 + s) * DM * 8, AUX_SC1); };
#pragma unroll
      for (int s = 0; s < 8; s++) gp[s] = ld2(s);
      settle<8>(gp, tag, ld2);
      const unsigned at = cvtpk(sum8(gp, 0), sum8(gp, 2));
      hh = cvtpk(lo2f(xh) + lo2f(at), hi2f(xh) + hi2f(at));
      if (tid < 288) *(LAS f32x2*)(lds + L_HS + tid * 8) = f32x2{lo2f(hh), hi2f(hh)};
    }
    STAMP(9);
    LDS_BARRIER();
    if (wave == 7) {
      if (fb == 0 && lane == 0) __builtin_amdgcn_raw_buffer_store_b32(tag, rws, 0, WS_EPOCH, AUX_SC1);
    } else if (wave < 5) {
      float v[9];
#pragma unroll
      for (int i = 0; i < 9; i++) v[i] = *(LAS float*)(lds + L_HS + (lane + 64 * i) * 4);
      const float mean = wave_sum(((v[0] + v[1]) + (v[2] + v[3])) + ((v[4] + v[5]) + (v[6] + v[7])) + v[8]) * (1.f / DM);
      float s2a = 0.f, s2b = 0.f;
#pragma unroll
      for (int i = 0; i < 9; i++) { const float a = v[i] - mean; if (i & 1) s2b = fmaf(a, a, s2b); else s2a = fmaf(a, a, s2a); }
      const float var = wave_sum(s2a + s2b) * (1.f / DM);
      const float rden = rden_of(var), mb = rbf(mean);
      const float mb2 = rbf((mean - mb) * rden), rden2 = rden_of(var * rden * rden);
      unsigned u = cvtpk(lo2f(hh) - mb, hi2f(hh) - mb);
      u = cvtpk(lo2f(u) * rden, hi2f(u) * rden);
      u = cvtpk(lo2f(u) - mb2, hi2f(u) - mb2);
      u = cvtpk(lo2f(u) * rden2, hi2f(u) * rden2);
      STAMP(10);
      const unsigned m0 = (u & 0x7fffu) - TAB0, m1 = ((u >> 16) & 0x7fffu) - TAB0;
      const bool in0 = m0 < NTAB / 2, in1 = m1 < NTAB / 2;
      const unsigned t0 = in0 ? m0 + (u >> 15 & 1) * (NTAB / 2) : 0, t1 = in1 ? m1 + (u >> 31) * (NTAB / 2) : 0;
      u32x2 f0 = *(LAS u32x2*)(lds + L_TAB + t0 * 8), f1 = *(LAS u32x2*)(lds + L_TAB + t1 * 8);
      if (__builtin_amdgcn_ballot_w64(!in0 || !in1 || f0[1] != tag || f1[1] != tag) != 0) {
#pragma unroll 1
        for (unsigned spin = 0; spin < SPIN_CAP && __builtin_amdgcn_ballot_w64(f0[1] != tag || f1[1] != tag) != 0; spin++) {
          f0 = __builtin_amdgcn_raw_buffer_load_b64(rws, t0 * 8, WS_T, AUX_SC1);
          f1 = __builtin_amdgcn_raw_buffer_load_b64(rws, t1 * 8, WS_T, AUX_SC1);
        }
#pragma unroll 1
        for (int c = 0; c < 2; c++) {
          unsigned long long todo = __builtin_amdgcn_ballot_w64(c ? !in1 : !in0);
#pragma unroll 1
          while (todo) {
            const int l = __builtin_ctzll(todo);
            todo &= todo - 1;
            const unsigned ul = __builtin_amdgcn_readlane(u, l);
            const float h = c ? hi2f(ul) : lo2f(ul);
            f32x2 a = {0.f, 0.f};
#pragma unroll 1
            for (int j = 0; j < FF / 512; j++) {
              const u32x4 a1 = *(const u32x4*)(p.w1 + 512 * j + 8 * lane), a2 = *(const u32x4*)(p.b1 + 512 * j + 8 * lane), a3 = *(const u32x4*)(p.w2 + 512 * j + 8 * lane);
#pragma unroll
              for (int k = 0; k < 4; k++)
                a = term(f32x2{h, h}, f32x2{lo2f(a1[k]), hi2f(a1[k])} * KS, f32x2{lo2f(a2[k]), hi2f(a2[k])} * KS, f32x2{lo2f(a3[k]), hi2f(a3[k])} * (1.f / KS), a);
            }
            const float r = wave_sum(a[0] + a[1]);
            if (lane == l) { if (c) f1[0] = __float_as_uint(r); else f0[0] = __float_as_uint(r); }
          }
        }
      }
      const float b2 = lo2f(b2v);
      unsigned y = cvtpk(__uint_as_float(f0[0]), __uint_as_float(f1[0]));
      y = cvtpk(lo2f(y) + b2, hi2f(y) + b2);
      auto rout = __builtin_amdgcn_make_buffer_rsrc((void*)p.out, 0, 2 * DM * 2, 0x00020000);
      if (tid < 288) __builtin_amdgcn_raw_buffer_store_b32(cvtpk(lo2f(hh) + lo2f(y), hi2f(hh) + hi2f(y)), rout, tid * 4, fb * DM * 2, AUX_SC1);
      STAMP(11);
    }
  }
  STAMP(12);
  asm volatile(".Lfused_last:");
}

extern "C" void kernel_launch(void* const* d_in, const int* in_sizes, int n_in, void* d_out, int out_size, void* d_ws, size_t ws_size, hipStream_t stream) {
  Params p;
  p.x = (const u16*)d_in[0]; p.K = (const char*)d_in[1]; p.V = (const char*)d_in[2];
  p.wq = (const char*)d_in[3]; p.wo = (const char*)d_in[4];
  p.w1 = (const u16*)d_in[6]; p.b1 = (const u16*)d_in[7]; p.w2 = (const u16*)d_in[8]; p.b2 = (const u16*)d_in[9];
  p.out = (u16*)d_out; p.ws = (char*)d_ws;
  fused_kernel<<<256, 512, 0, stream>>>(p);
}
```

```cpp
#include <hip/hip_runtime.h>
#include <hip/hip_bf16.h>
#include <stdint.h>

#define DIAG 0
#define TOUCH0 1
#define RPERM 0
#define SLEEP 1
#define WOPOS 1
#define NPT 3
#define PLACE 0
#define XT 0
#define TK 0
#define TV 0
#define TN 2
#define WLATE 0
#define FIN0 254
#define BSTEP 8
#define PERM 21,16,9,10,8,20,17,18,14,19,13,15,12,22,11
#define BAR0 1

typedef unsigned u32x4 __attribute__((ext_vector_type(4)));
typedef unsigned u32x3 __attribute__((ext_vector_type(3)));
typedef unsigned u32x2 __attribute__((ext_vector_type(2)));
typedef float f32x4 __attribute__((ext_vector_type(4)));
typedef float f32x2 __attribute__((ext_vector_type(2)));
typedef __bf16 bf16x8 __attribute__((ext_vector_type(8)));
typedef __bf16 bf16x2 __attribute__((ext_vector_type(2)));
typedef short s16x4 __attribute__((ext_vector_type(4)));
typedef unsigned short u16;
#define LAS __attribute__((address_space(3)))

constexpr int DM = 576, FF = 4608;
constexpr float LOG2E = 1.4426950408889634f;
constexpr float SCALE = 0.08838834764831845f;
constexpr int NSL = 8;
constexpr int NTURN = 32 / NSL;
constexpr int NP = 8 * NPT;
constexpr int NTAB = 256 * NP;
constexpr unsigned TAB0 = (NPT == 3 ? 127 - 19 : 127 - 13) << 7;
constexpr unsigned SPIN_CAP = 1u << 16;
constexpr int AUX_NT = 2, AUX_SC1 = 16;

constexpr unsigned WS_EPOCH = 0;
constexpr unsigned WS_QG = 4096;
constexpr unsigned WS_P1 = WS_QG + 32 * 8 * 2048;
constexpr unsigned WS_ML = WS_P1 + 512 * 1024;
constexpr unsigned WS_P2 = WS_ML + 512 * 16;
constexpr unsigned WS_T = WS_P2 + 16 * DM * 8;
constexpr unsigned WS_END = WS_T + NTAB * 8;
constexpr unsigned WS_STAMP = 2u << 20;

constexpr int L_Q = 0;
constexpr int L_ML = 512;
constexpr int L_O = 640;
constexpr int L_OL = L_O + 8192;
constexpr int L_HS = L_OL + 2 * 1088;
constexpr int L_RED = L_HS + DM * 4;
constexpr int L_WO = L_RED + NP * 32;
constexpr int L_TAB = L_WO + 32768;
constexpr int L_END = L_TAB + NTAB * 8;

struct Params {
  const u16* x; const char* K; const char* V; const char* wq; const char* wo;
  const u16* w1; const u16* b1; const u16* w2; const u16* b2; u16* out; char* ws;
};

__device__ __forceinline__ float lo2f(unsigned v) { return __uint_as_float(v << 16); }
__device__ __forceinline__ float hi2f(unsigned v) { return __uint_as_float(v & 0xffff0000u); }
__device__ __forceinline__ unsigned cvtpk(float lo, float hi) {
  f32x2 v = {lo, hi};
  return __builtin_bit_cast(unsigned, __builtin_convertvector(v, bf16x2));
}
__device__ __forceinline__ float rbf(float x) { return lo2f(cvtpk(x, x)); }
template <int CTRL> __device__ __forceinline__ float dpp(float x) {
  return __builtin_bit_cast(float, __builtin_amdgcn_mov_dpp(__builtin_bit_cast(int, x), CTRL, 0xf, 0xf, true));
}
constexpr int XOR1 = 0xB1, XOR2 = 0x4E, HMIR = 0x141, MIR = 0x140;
__device__ __forceinline__ float row_sum(float x) {
  x += dpp<XOR1>(x); x += dpp<XOR2>(x); x += dpp<HMIR>(x); x += dpp<MIR>(x); return x;
}
__device__ __forceinline__ float x16_add(float a, float b) {
  auto s = __builtin_amdgcn_permlane16_swap(__float_as_uint(a), __float_as_uint(b), false, false);
  return __uint_as_float(s[0]) + __uint_as_float(s[1]);
}
__device__ __forceinline__ float x32_add(float a, float b) {
  auto s = __builtin_amdgcn_permlane32_swap(__float_as_uint(a), __float_as_uint(b), false, false);
  return __uint_as_float(s[0]) + __uint_as_float(s[1]);
}
__device__ __forceinline__ float x16_max(float x) {
  auto s = __builtin_amdgcn_permlane16_swap(__float_as_uint(x), __float_as_uint(x), false, false);
  return fmaxf(__uint_as_float(s[0]), __uint_as_float(s[1]));
}
__device__ __forceinline__ float x32_max(float x) {
  auto s = __builtin_amdgcn_permlane32_swap(__float_as_uint(x), __float_as_uint(x), false, false);
  return fmaxf(__uint_as_float(s[0]), __uint_as_float(s[1]));
}
__device__ __forceinline__ float wave_sum(float x) { x = row_sum(x); x = x16_add(x, x); return x32_add(x, x); }
__device__ __forceinline__ float rdlane(float x, int l) { return __builtin_bit_cast(float, __builtin_amdgcn_readlane(__builtin_bit_cast(int, x), l)); }
__device__ __forceinline__ float rden_of(float var) {
  return __builtin_amdgcn_rcpf(rbf(__builtin_amdgcn_sqrtf(rbf(rbf(var) + 1e-5f))));
}
__device__ __forceinline__ f32x4 mfma(u32x4 a, u32x4 b, f32x4 c) {
  return __builtin_amdgcn_mfma_f32_16x16x32_bf16(__builtin_bit_cast(bf16x8, a), __builtin_bit_cast(bf16x8, b), c, 0, 0, 0);
}
__device__ __forceinline__ u32x4 read_tr(LAS char* a, LAS char* b) {
  const u32x2 ua = __builtin_bit_cast(u32x2, __builtin_amdgcn_ds_read_tr16_b64_v4i16((LAS s16x4*)a));
  const u32x2 ub = __builtin_bit_cast(u32x2, __builtin_amdgcn_ds_read_tr16_b64_v4i16((LAS s16x4*)b));
  return u32x4{ua[0], ua[1], ub[0], ub[1]};
}
#define SB() __builtin_amdgcn_sched_barrier(0)
#define PIN(x) do { asm volatile("" : "+v"(x) :: "memory"); __builtin_amdgcn_sched_barrier(0); } while (0)
#define PINM() do { asm volatile("" ::: "memory"); __builtin_amdgcn_sched_barrier(0); } while (0)
#define SINK(x) asm volatile("" :: "v"(x))
#define VMWAIT(n) do { __builtin_amdgcn_sched_barrier(0); asm volatile("s_waitcnt vmcnt(" #n ")" ::: "memory"); __builtin_amdgcn_sched_barrier(0); } while (0)
#define LDS_BARRIER() do { asm volatile("s_waitcnt lgkmcnt(0)" ::: "memory"); __builtin_amdgcn_s_barrier(); asm volatile("" ::: "memory"); } while (0)
#define STAMP(i) do {} while (0)

constexpr float KS = 0.4686686930532063f;
constexpr float BETA = 2.f * LOG2E * 0.7978845608028654f / KS;
__device__ __forceinline__ f32x2 term(f32x2 h, f32x2 w1, f32x2 b1, f32x2 w2, f32x2 a) {
  const f32x2 v = __builtin_elementwise_fma(h, w1, b1);
  const f32x2 z = v * __builtin_elementwise_fma(v, v, f32x2{BETA, BETA});
  const f32x2 d = f32x2{__builtin_amdgcn_exp2f(-z[0]), __builtin_amdgcn_exp2f(-z[1])} + 1.f;
  return __builtin_elementwise_fma(v * w2, f32x2{__builtin_amdgcn_rcpf(d[0]), __builtin_amdgcn_rcpf(d[1])}, a);
}
template <int V> struct IC { static constexpr int value = V; };
template <int N, class F>
__device__ __forceinline__ void settle(u32x4 (&g)[N], unsigned tag, F load) {
#pragma unroll 1
  for (unsigned spin = 0; spin < SPIN_CAP; spin++) {
    unsigned y[N], any = 0;
#pragma unroll
    for (int s = 0; s < N; s++) { y[s] = __builtin_amdgcn_bitop3_b32(g[s][1], g[s][3], tag, 0x7e); any |= y[s]; }
    if (__builtin_amdgcn_ballot_w64(any != 0) == 0) break;
    __builtin_amdgcn_s_sleep(SLEEP);
    asm volatile("" ::: "memory");
#pragma unroll
    for (int s = 0; s < N; s++)
      if (__builtin_amdgcn_ballot_w64(y[s] != 0) != 0) g[s] = load(s);
  }
}
__device__ __forceinline__ float fadd(float a, float b) { float r; asm("v_add_f32 %0, %1, %2" : "=v"(r) : "v"(a), "v"(b)); return r; }
__device__ __forceinline__ float sum8(const u32x4 (&g)[8], int c) {
  float t[4];
#pragma unroll
  for (int i = 0; i < 4; i++) t[i] = fadd(__uint_as_float(g[2 * i][c]), __uint_as_float(g[2 * i + 1][c]));
  return fadd(fadd(t[0], t[1]), fadd(t[2], t[3]));
}

__global__ void __launch_bounds__(512, 2) fused_kernel(Params p) {
  __shared__ __attribute__((aligned(16))) char lds_[L_END];
  LAS char* const lds = (LAS char*)lds_;
  const int tid = threadIdx.x, lane = tid & 63;
  const int wave = __builtin_amdgcn_readfirstlane(tid >> 6);
  const int bid = blockIdx.x;
  const int g = lane >> 4, n = lane & 15;
  STAMP(0);
  auto rws = __builtin_amdgcn_make_buffer_rsrc((void*)p.ws, 0, WS_END, 0x00020000);
  auto rk = __builtin_amdgcn_make_buffer_rsrc((void*)p.K, 0, 256u << 20, 0x00020000);
  auto rv = __builtin_amdgcn_make_buffer_rsrc((void*)p.V, 0, 256u << 20, 0x00020000);
  char* pc; unsigned pclen;
  asm volatile("s_getpc_b64 %0\n.Lfused_first:\n\ts_mov_b32 %1, .Lfused_last-.Lfused_first" : "=s"(pc), "=s"(pclen));
  auto rc = __builtin_amdgcn_make_buffer_rsrc((void*)pc, 0, pclen, 0x00020000);
  unsigned epoch = *(const __attribute__((address_space(4))) unsigned*)(p.ws + WS_EPOCH);

  const int ab = bid >> 7, kvh = (bid >> 3) & 15, sp = bid & 7;
  constexpr int PB[15] = {PERM};
  auto sprd = [&](unsigned v, int first, int nb) { unsigned r = 0; for (int i = 0; i < nb; i++) r |= ((v >> i) & 1u) << PB[first + i]; return r; };
  unsigned va[4];
#pragma unroll
  for (int t = 0; t < 4; t++) va[t] = sprd(g + 4 * t, 0, 4) + n * 16;
  const unsigned sbase = ((unsigned)(bid >> 3) << 23) + sprd(wave, 4, 3);
  auto turn_off = [&](int i) { return sbase + sprd(i, 10, 2) + sprd(sp ^ (XT >> (4 * (i & 3))), 12, 3); };
  unsigned so = turn_off(0);
#define SO(s) (so + sprd(s, 7, 3))
  u32x4 rb[NSL][4];
#define LOAD(s, rs) _Pragma("unroll") for (int t = 0; t < 4; t++) rb[s][t] = __builtin_amdgcn_raw_buffer_load_b128(rs, va[t], SO(s), AUX_NT)

  unsigned msk[4];
  auto mkmask = [&]() {
    int nn = n;
    asm volatile("" : "+v"(nn));
#pragma unroll
    for (int k = 0; k < 4; k++) msk[k] = (((nn & 7) >> 1) == k) ? ((nn & 1) ? 0xffff0000u : 0x0000ffffu) : 0u;
  };

  unsigned tk[TN], tv[TN];
  auto pages = [&](const char* base, unsigned (&d)[TN]) {
    const char* a = base + (((unsigned)(bid >> 3) << 23) + (sprd(sp, 12, 3) & (1u << 22)));
#pragma unroll
    for (int i = 0; i < TN; i++) asm volatile("s_load_dword %0, %1, %2" : "=s"(d[i]) : "s"(a), "s"(i * ((4u << 20) / TN)) : "memory");
  };
  auto landed = [&](unsigned (&d)[TN]) {
    asm volatile("s_waitcnt lgkmcnt(0)" ::: "memory");
#pragma unroll
    for (int i = 0; i < TN; i++) asm volatile("" :: "s"(d[i]));
  };
  if (TK && wave == 1) pages(p.K, tk);
  if (TV == 1 && wave == 2) pages(p.V, tv);
  PINM();
  unsigned touch = 0;
  touch = __builtin_amdgcn_raw_buffer_load_b32(rc, lane * 64, ((wave + 7) & 7) * 4096, 0);
  PINM();
  if (wave == 0) {
    const int hq = bid >> 3, ks = bid & 7;
    auto rx = __builtin_amdgcn_make_buffer_rsrc((void*)p.x, 0, 2 * DM * 2, 0x00020000);
    auto rq = __builtin_amdgcn_make_buffer_rsrc((void*)p.wq, 0, DM * 4096 * 2, 0x00020000);
    u32x3 xs[3], xm[3];
    u32x4 wb[18];
#pragma unroll
    for (int i = 0; i < 3; i++) xs[i] = __builtin_amdgcn_raw_buffer_load_b96(rx, lane * 36 + 12 * i, 0, 0);
#pragma unroll
    for (int i = 0; i < 3; i++) xm[i] = __builtin_amdgcn_raw_buffer_load_b96(rx, ((n >> 3) * 32 + 4 * ks + g) * 36 + 12 * i, 0, 0);
#pragma unroll
    for (int t = 0; t < 18; t++) wb[t] = __builtin_amdgcn_raw_buffer_load_b128(rq, g * 18 * 8192 + n * 16, (72 * ks + t) * 8192 + hq * 256, AUX_NT);
    PINM();
    asm volatile("s_barrier" ::: "memory");
    PINM();
    mkmask();
    float s1 = 0.f;
#pragma unroll
    for (int i = 0; i < 9; i++) { const unsigned u = xs[i / 3][i % 3]; s1 += lo2f(u) + hi2f(u); }
    s1 = row_sum(s1);
    const float mean = x16_add(s1, s1) * (1.f / DM);
    float s2 = 0.f;
#pragma unroll
    for (int i = 0; i < 9; i++) { const unsigned u = xs[i / 3][i % 3]; const float a = lo2f(u) - mean, c = hi2f(u) - mean; s2 += a * a + c * c; }
    s2 = row_sum(s2);
    const float rden = rden_of(x16_add(s2, s2) * (1.f / DM)), mb = rbf(mean);
    const float mb0 = rdlane(mb, 0), mb1 = rdlane(mb, 32), rd0 = rdlane(rden, 0), rd1 = rdlane(rden, 32);
    const float mb_ = (n < 8) ? mb0 : mb1, rd_ = (n < 8) ? rd0 : rd1;
    f32x4 qa = {0, 0, 0, 0};
#pragma unroll
    for (int i = 0; i < 9; i++) {
      const unsigned u = xm[i / 3][i % 3], d = cvtpk(lo2f(u) - mb_, hi2f(u) - mb_);
      const float y0 = lo2f(d) * rd_, y1 = hi2f(d) * rd_;
      const unsigned p0 = cvtpk(y0, y0), p1 = cvtpk(y1, y1);
      qa = mfma(u32x4{p0 & msk[0], p0 & msk[1], p0 & msk[2], p0 & msk[3]}, wb[2 * i], qa);
      qa = mfma(u32x4{p1 & msk[0], p1 & msk[1], p1 & msk[2], p1 & msk[3]}, wb[2 * i + 1], qa);
    }
    unsigned tag = epoch;
    asm volatile("" : "+s"(tag));
    tag += 1;
    __builtin_amdgcn_raw_buffer_store_b128(u32x4{__float_as_uint(qa[0]), tag, __float_as_uint(qa[1]), tag}, rws, lane * 16, WS_QG + bid * 2048, AUX_SC1);
    __builtin_amdgcn_raw_buffer_store_b128(u32x4{__float_as_uint(qa[2]), tag, __float_as_uint(qa[3]), tag}, rws, lane * 16, WS_QG + bid * 2048 + 1024, AUX_SC1);
    STAMP(1);
  }
  else asm volatile("s_barrier" ::: "memory");
  PINM();
#pragma unroll
  for (int t = 0; t < 4; t++) PIN(va[t]);
#pragma unroll
  for (int s = 0; s < NSL; s++) { LOAD(s, rk); PINM(); }
  asm volatile("" : "+s"(epoch));
  const unsigned tag = epoch + 1;
  const bool fin = bid >= FIN0 && bid < FIN0 + 2;
  const int fb = bid & 1;
  u32x4 w1v, b1v, w2v; unsigned w1t, b1t, w2t, xh, b2v;
  auto ldw = [&]() {
    w1v = *(const u32x4*)(p.w1 + 8 * tid); b1v = *(const u32x4*)(p.b1 + 8 * tid); w2v = *(const u32x4*)(p.w2 + 8 * tid);
    w1t = p.w1[4096 + tid]; b1t = p.b1[4096 + tid]; w2t = p.w2[4096 + tid];
    xh = *(const unsigned*)(p.x + fb * DM + 2 * min(tid, 287)); b2v = p.b2[0];
    PINM();
  };
  if (!WLATE) ldw();
  if (TV == 2 && wave == 3) pages(p.V, tv);
  PINM();
  unsigned kw[4], kr[4];
#pragma unroll
  for (int t = 0; t < 4; t++) {
    kw[t] = L_WO + wave * 4096 + (4 * g + t) * 256 + ((n ^ (4 * g + t)) << 4);
    kr[t] = L_WO + wave * 4096 + n * 256 + (((4 * t + g) ^ n) << 4);
    PIN(kw[t]); PIN(kr[t]);
  }
  PINM();
  if (wave == 1 || wave == 2) {
    u32x4 pq[8];
    auto ldq = [&](int s) {
      return __builtin_amdgcn_raw_buffer_load_b128(rws, (lane >> 5) * 1024 + (32 * ab + (lane & 31)) * 16, WS_QG + ((2 * kvh + wave - 1) * 8 + s) * 2048, AUX_SC1);
    };
#pragma unroll
    for (int s = 0; s < 8; s++) pq[s] = ldq(s);
    STAMP(14);
    settle<8>(pq, tag, ldq);
    const float qa = sum8(pq, 0), qc = sum8(pq, 2);
    *(LAS unsigned*)(lds + L_Q + (wave - 1) * 256 + (8 * n + 4 * (g & 1) + 2 * (lane >> 5)) * 2) = cvtpk(qa, qc);
    STAMP(2);
  }
  SINK(touch);
  if (TK) landed(tk);
  if (TV) landed(tv);
  LDS_BARRIER();
  u32x4 qf[4];
#pragma unroll
  for (int s = 0; s < 4; s++) qf[s] = *(LAS u32x4*)(lds + L_Q + (n >> 3) * 256 + (32 * s + 8 * g) * 2);
  STAMP(3);

  unsigned Z[NTURN][2];
  f32x2 W1[4], B1[4], W2[4];
  float W1t, B1t, W2t, ya[4];
  auto point2 = [&](int k, unsigned t) {
    const unsigned hb = t >= NTAB / 2 ? t - NTAB / 2 + TAB0 + 0x8000u : t + TAB0;
    const float h0 = lo2f(hb), h1 = lo2f(hb + 1);
    const f32x2 tt = term(f32x2{h0, h1}, f32x2{W1t, W1t}, f32x2{B1t, B1t}, f32x2{W2t, W2t}, f32x2{0.f, 0.f});
    f32x2 a = {tt[0], 0.f}, c = {tt[1], 0.f};
#pragma unroll
    for (int j = 0; j < 4; j++) { a = term(f32x2{h0, h0}, W1[j], B1[j], W2[j], a); c = term(f32x2{h1, h1}, W1[j], B1[j], W2[j], c); }
    ya[k] = a[0] + a[1]; ya[k + 1] = c[0] + c[1];
    PIN(ya[k]); PIN(ya[k + 1]);
  };
  constexpr int PT = WLATE ? NP / 2 : 8, T0 = WLATE ? 2 : 1;
  auto kturn = [&](auto tab_, auto rs, int i) {
    constexpr bool tab = decltype(tab_)::value;
    const bool on = WLATE || NPT == NTURN - 1 || i <= NPT;
    unsigned z0 = 0, z1 = 0;
#pragma unroll
    for (int s = 0; s < NSL; s++) {
      if (s % BSTEP == 0) { SB(); __builtin_amdgcn_s_barrier(); SB(); }
#pragma unroll
      for (int t = 0; t < 4; t++) *(LAS u32x4*)(lds + kw[t]) = rb[s][t];
      PINM(); LOAD(s, rs); PINM();
      f32x4 sc = {0, 0, 0, 0};
#pragma unroll
      for (int t = 0; t < 4; t++) sc = mfma(*(LAS u32x4*)(lds + kr[t]), qf[t], sc);
      const unsigned u0 = cvtpk(sc[0], sc[1]), u1 = cvtpk(sc[2], sc[3]);
      z0 = (n & 7) == s ? u0 : z0; z1 = (n & 7) == s ? u1 : z1;
      PIN(z0); PIN(z1);
      if (tab && on) {
        const int k0 = (PLACE == 1 ? 0 : s * PT / 16) * 2, k1 = (PLACE == 1 ? (s == 7) * PT / 2 : (s + 1) * PT / 16) * 2;
#pragma unroll
        for (int k = k0; k < k1; k += 2) {
          point2(k & 3, bid * NP + PT * (i - T0) + k);
          if (k & 2) {
            const float r = row_sum(x16_add(x32_add(ya[0], ya[1]), x32_add(ya[2], ya[3])));
            if (n == 0) *(LAS float*)(lds + L_RED + (PT * (i - T0) + (k & ~3) + (g >> 1) + 2 * (g & 1)) * 32 + wave * 4) = r;
            PINM();
          }
        }
      }
    }
#pragma unroll
    for (int j = 0; j < NTURN - 1; j++) { Z[j][0] = Z[j + 1][0]; Z[j][1] = Z[j + 1][1]; }
    Z[NTURN - 1][0] = z0; Z[NTURN - 1][1] = z1;
  };
#pragma unroll 1
  for (int i = 0; i < T0; i++) {
    so = turn_off(i + 1);
    if (WLATE && i == T0 - 1) ldw();
    kturn(IC<0>{}, rk, i);
  }
#pragma unroll
  for (int j = 0; j < 4; j++) {
    W1[j] = f32x2{lo2f(w1v[j]), hi2f(w1v[j])} * KS;
    B1[j] = f32x2{lo2f(b1v[j]), hi2f(b1v[j])} * KS;
    W2[j] = f32x2{lo2f(w2v[j]), hi2f(w2v[j])} * (1.f / KS);
    PIN(W1[j]); PIN(B1[j]); PIN(W2[j]);
  }
  W1t = lo2f(w1t) * KS; B1t = lo2f(b1t) * KS; W2t = lo2f(w2t) * (1.f / KS);
  PIN(W1t); PIN(B1t); PIN(W2t);
#pragma unroll 1
  for (int i = T0; i < NTURN; i++) {
    const bool last = i == NTURN - 1;
    so = turn_off(i + 1);
    kturn(IC<1>{}, __builtin_amdgcn_make_buffer_rsrc((void*)(last ? p.V : p.K), 0, 256u << 20, 0x00020000), i);
  }
  STAMP(15);
  unsigned sel[2][4];
  {
    float z[4 * NTURN];
#pragma unroll
    for (int j = 0; j < 2 * NTURN; j++) {
      const unsigned u = Z[j >> 1][j & 1], w = cvtpk(lo2f(u) * SCALE, hi2f(u) * SCALE);
      z[2 * j] = lo2f(w); z[2 * j + 1] = hi2f(w);
    }
    float mx = z[0];
#pragma unroll
    for (int j = 1; j < 4 * NTURN; j++) mx = fmaxf(mx, z[j]);
    mx = fmaxf(mx, dpp<XOR1>(mx)); mx = fmaxf(mx, dpp<XOR2>(mx)); mx = fmaxf(mx, dpp<HMIR>(mx));
    mx = x32_max(x16_max(mx));
    if (lane == 0 || lane == 8) *(LAS float*)(lds + L_ML + (n >> 3) * 32 + wave * 4) = mx;
#pragma unroll
    for (int k = 0; k < 4; k++) {
      const bool on = ((n & 7) >> 1) == k;
      sel[0][k] = on ? ((n & 1) ? 0x01000c0cu : 0x0c0c0100u) : 0x0c0c0c0cu;
      sel[1][k] = on ? ((n & 1) ? 0x03020c0cu : 0x0c0c0302u) : 0x0c0c0c0cu;
    }
    LDS_BARRIER();
    if (wave == 1 && lane < NP) {
      const f32x4 a = *(LAS f32x4*)(lds + L_RED + lane * 32), c = *(LAS f32x4*)(lds + L_RED + lane * 32 + 16);
      __builtin_amdgcn_raw_buffer_store_b64(u32x2{__float_as_uint(((a[0] + a[1]) + (a[2] + a[3])) + ((c[0] + c[1]) + (c[2] + c[3]))), tag}, rws, lane * 8, WS_T + bid * NP * 8, AUX_SC1);
    }
    PINM();
    const f32x4 ma = *(LAS f32x4*)(lds + L_ML + (n >> 3) * 32), mb = *(LAS f32x4*)(lds + L_ML + (n >> 3) * 32 + 16);
    const float m2 = fmaxf(fmaxf(fmaxf(ma[0], ma[1]), fmaxf(ma[2], ma[3])), fmaxf(fmaxf(mb[0], mb[1]), fmaxf(mb[2], mb[3]))) * LOG2E;
    float l = 0.f;
#pragma unroll
    for (int j = 0; j < 2 * NTURN; j++) {
      const float ea = __builtin_amdgcn_exp2f(fmaf(z[2 * j], LOG2E, -m2)), eb = __builtin_amdgcn_exp2f(fmaf(z[2 * j + 1], LOG2E, -m2));
      l += ea + eb;
      Z[j >> 1][j & 1] = cvtpk(ea, eb);
      asm volatile("" : "+v"(Z[j >> 1][j & 1]));
    }
    l += dpp<XOR1>(l); l += dpp<XOR2>(l); l += dpp<HMIR>(l);
    l = x16_add(l, l); l = x32_add(l, l);
    if (lane == 0 || lane == 8) *(LAS float*)(lds + L_ML + 64 + (n >> 3) * 32 + wave * 4) = l;
    LDS_BARRIER();
    if (wave == 0) {
      const f32x4 la = *(LAS f32x4*)(lds + L_ML + 64 + (n >> 3) * 32), lb = *(LAS f32x4*)(lds + L_ML + 64 + (n >> 3) * 32 + 16);
      const float L = ((la[0] + la[1]) + (la[2] + la[3])) + ((lb[0] + lb[1]) + (lb[2] + lb[3]));
      if (lane == 0 || lane == 8)
        __builtin_amdgcn_raw_buffer_store_b128(u32x4{__float_as_uint(m2), tag, __float_as_uint(L), tag}, rws, (n >> 3) * 128, WS_ML + ((ab * 32 + 2 * kvh) * 8 + sp) * 16, AUX_SC1);
    }
  }
  const int rho = bid & 7, cg = bid >> 3;
  auto ldwo = [&](int row0, int cnt) {
    const u32x4 ro = {(unsigned)(uintptr_t)p.wo, (unsigned)((uintptr_t)p.wo >> 32) & 0xffffu, 4096 * DM * 2, 0x00020000};
    const unsigned vo = (lane >> 2) * 1152 + (((lane & 3) ^ ((lane >> 4) & 2)) << 4);
#pragma unroll
    for (int t = 0; t < cnt; t++)
      asm volatile("s_mov_b32 m0, %0\n\ts_nop 0\n\tbuffer_load_dwordx4 %1, %2, %3 offen nt lds"
                   :: "s"((unsigned)(uintptr_t)lds + L_WO + (row0 + 16 * t) * 64), "v"(vo), "s"(ro), "s"((512 * rho + row0 + 16 * t) * 1152 + 64 * cg) : "memory");
  };
  PINM();
  f32x4 acc = {0, 0, 0, 0};
  auto pv = [&](auto sc_, const unsigned (&P)[2], const u32x4 (&v)[4]) {
    constexpr int s = decltype(sc_)::value;
#pragma unroll
    for (int h = 0; h < 2; h++) {
      int b = __builtin_amdgcn_mov_dpp((int)P[h], 0x150 + s, 0xf, 0xf, true);
      b = __builtin_amdgcn_update_dpp(b, (int)P[h], 0x158 + s, 0xf, 0xc, false);
#pragma unroll
      for (int c = 0; c < 2; c++)
        acc = mfma(u32x4{__builtin_amdgcn_perm(b, b, sel[c][0]), __builtin_amdgcn_perm(b, b, sel[c][1]), __builtin_amdgcn_perm(b, b, sel[c][2]), __builtin_amdgcn_perm(b, b, sel[c][3])}, v[2 * h + c], acc);
    }
  };
  u32x4 gm[1];
  auto rml = __builtin_amdgcn_make_buffer_rsrc((void*)p.ws, 0, wave < 2 ? WS_END : 0, 0x00020000);
  auto ldm = [&](int) { return __builtin_amdgcn_raw_buffer_load_b128(rml, (lane & 7) * 16, WS_ML + (ab * 32 + 2 * kvh + wave) * 128, AUX_SC1); };
#define FOR8(X) X(0) X(1) X(2) X(3) X(4) X(5) X(6) X(7)
#pragma unroll 1
  for (int i = 0; i < NTURN - 1; i++) {
    so = turn_off(i + 1);
    gm[0] = ldm(0);
    if (fin && i == NTURN - 2) {
      const u32x4 rt = {(unsigned)(uintptr_t)p.ws, (unsigned)((uintptr_t)p.ws >> 32) & 0xffffu, WS_END, 0x00020000};
#pragma unroll
      for (int t = 0; t < NP / 4; t++)
        asm volatile("s_mov_b32 m0, %0\n\ts_nop 0\n\tbuffer_load_dwordx4 %1, %2, %3 offen sc1 lds"
                     :: "s"((unsigned)(uintptr_t)lds + L_TAB + (wave * (NP / 4) + t) * 1024), "v"(lane * 16), "s"(rt), "s"(WS_T + (wave * (NP / 4) + t) * 1024) : "memory");
    }
    PINM();
#define VSTEP(s) if (s % BSTEP == 0) { SB(); __builtin_amdgcn_s_barrier(); SB(); } pv(IC<s>{}, Z[0], rb[s]); PIN(acc); LOAD(s, rv); SB();
    FOR8(VSTEP)
#pragma unroll
    for (int j = 0; j < NTURN - 1; j++) { Z[j][0] = Z[j + 1][0]; Z[j][1] = Z[j + 1][1]; }
  }
  STAMP(4);
  const unsigned hrow = (wave >> 2) * 32 + 4 * rho + (wave & 3);
  auto ld1 = [&](int s) { return __builtin_amdgcn_raw_buffer_load_b128(rws, lane * 16, WS_P1 + hrow * 8192 + s * 1024, AUX_SC1); };
  u32x4 go[8], bfr[16];
  float wgt = 0.f;
  {
    __builtin_amdgcn_s_barrier();
#define VLAST(s) pv(IC<s>{}, Z[0], rb[s]); SB();
    FOR8(VLAST)
    PIN(acc);
    if (wave < 2) {
      settle<1>(gm, tag, ldm);
      const float ms = __uint_as_float(gm[0][0]);
      float M = fmaxf(ms, dpp<XOR1>(ms)); M = fmaxf(M, dpp<XOR2>(M)); M = fmaxf(M, dpp<HMIR>(M));
      const float e = __builtin_amdgcn_exp2f(ms - M);
      float L = e * __uint_as_float(gm[0][2]); L += dpp<XOR1>(L); L += dpp<XOR2>(L); L += dpp<HMIR>(L);
      wgt = rdlane(e * __builtin_amdgcn_rcpf(L), sp);
    }
    PINM();
    if (wave < 4 && cg < 18) ldwo(128 * wave, 8);
    SB();
  }
  STAMP(5);
  *(LAS f32x4*)(lds + L_O + wave * 1024 + (g >> 1) * 512 + (8 * n + 4 * (g & 1)) * 4) = acc;
  LDS_BARRIER();
  STAMP(13);
  if (wave < 2) {
    f32x2 o[8];
#pragma unroll
    for (int w = 0; w < 8; w++) o[w] = *(LAS f32x2*)(lds + L_O + w * 1024 + wave * 512 + lane * 8);
    const f32x2 t = (((o[0] + o[1]) + (o[2] + o[3])) + ((o[4] + o[5]) + (o[6] + o[7]))) * wgt;
    __builtin_amdgcn_raw_buffer_store_b128(u32x4{__float_as_uint(t[0]), tag, __float_as_uint(t[1]), tag}, rws, lane * 16, WS_P1 + ((ab * 32 + 2 * kvh + wave) * 8 + sp) * 1024, AUX_SC1);
  }
  STAMP(6);
  if (cg < 18) {
#pragma unroll
    for (int s = 0; s < 8; s++) go[s] = ld1(s);
    unsigned olw = L_OL + (wave >> 2) * 1088 + (wave & 3) * 256 + lane * 4, olr = L_OL + (n & 1) * 1088 + 16 * g, p2o = (g * 8 * DM + n) * 8;
    PIN(olw); PIN(olr); PIN(p2o);
    VMWAIT(8);
    __builtin_amdgcn_s_barrier();
    PINM();
    if (wave < 2) {
#pragma unroll
      for (int s = 0; s < 16; s++) {
        const int a = L_WO + (32 * s + 8 * g + (n >> 2)) * 64 + ((wave ^ (g & 1)) << 5) + (n & 3) * 8;
        bfr[s] = read_tr(lds + a, lds + a + 256);
      }
    }
    settle<8>(go, tag, ld1);
    STAMP(7);
    const float o0 = sum8(go, 0), o1 = sum8(go, 2);
    *(LAS unsigned*)(lds + olw) = cvtpk(o0, o1);
    LDS_BARRIER();
    if (wave < 2) {
      u32x4 af[16];
#pragma unroll
      for (int s = 0; s < 16; s++) af[s] = *(LAS u32x4*)(lds + olr + 64 * s);
      SB();
      f32x4 d[4] = {};
#pragma unroll
      for (int s = 0; s < 16; s++) d[s & 3] = mfma(af[s], bfr[s], d[s & 3]);
      const float r = g ? (d[0][1] + d[1][1]) + (d[2][1] + d[3][1]) : (d[0][0] + d[1][0]) + (d[2][0] + d[3][0]);
      if (g < 2) __builtin_amdgcn_raw_buffer_store_b64(u32x2{__float_as_uint(r), tag}, rws, p2o, WS_P2 + (rho * DM + 32 * cg + 16 * wave) * 8, AUX_SC1);
    }
    STAMP(8);
  }
  PINM();
  if (fin) {
    unsigned hh = 0;
    if (wave < 5) {
      u32x4 gp[8];
      auto ld2 = [&](int s) { return __builtin_amdgcn_raw_buffer_load_b128(rws, min(tid, 287) * 16, WS_P2 + (fb * 8 + s) * DM * 8, AUX_SC1); };
#pragma unroll
      for (int s = 0; s < 8; s++) gp[s] = ld2(s);
      settle<8>(gp, tag, ld2);
      const unsigned at = cvtpk(sum8(gp, 0), sum8(gp, 2));
      hh = cvtpk(lo2f(xh) + lo2f(at), hi2f(xh) + hi2f(at));
      if (tid < 288) *(LAS f32x2*)(lds + L_HS + tid * 8) = f32x2{lo2f(hh), hi2f(hh)};
    }
    STAMP(9);
    LDS_BARRIER();
    if (wave == 7) {
      if (fb == 0 && lane == 0) __builtin_amdgcn_raw_buffer_store_b32(tag, rws, 0, WS_EPOCH, AUX_SC1);
    } else if (wave < 5) {
      float v[9];
#pragma unroll
      for (int i = 0; i < 9; i++) v[i] = *(LAS float*)(lds + L_HS + (lane + 64 * i) * 4);
      const float mean = wave_sum(((v[0] + v[1]) + (v[2] + v[3])) + ((v[4] + v[5]) + (v[6] + v[7])) + v[8]) * (1.f / DM);
      float s2a = 0.f, s2b = 0.f;
#pragma unroll
      for (int i = 0; i < 9; i++) { const float a = v[i] - mean; if (i & 1) s2b = fmaf(a, a, s2b); else s2a = fmaf(a, a, s2a); }
      const float var = wave_sum(s2a + s2b) * (1.f / DM);
      const float rden = rden_of(var), mb = rbf(mean);
      const float mb2 = rbf((mean - mb) * rden), rden2 = rden_of(var * rden * rden);
      unsigned u = cvtpk(lo2f(hh) - mb, hi2f(hh) - mb);
      u = cvtpk(lo2f(u) * rden, hi2f(u) * rden);
      u = cvtpk(lo2f(u) - mb2, hi2f(u) - mb2);
      u = cvtpk(lo2f(u) * rden2, hi2f(u) * rden2);
      STAMP(10);
      const unsigned m0 = (u & 0x7fffu) - TAB0, m1 = ((u >> 16) & 0x7fffu) - TAB0;
      const bool in0 = m0 < NTAB / 2, in1 = m1 < NTAB / 2;
      const unsigned t0 = in0 ? m0 + (u >> 15 & 1) * (NTAB / 2) : 0, t1 = in1 ? m1 + (u >> 31) * (NTAB / 2) : 0;
      u32x2 f0 = *(LAS u32x2*)(lds + L_TAB + t0 * 8), f1 = *(LAS u32x2*)(lds + L_TAB + t1 * 8);
      if (__builtin_amdgcn_ballot_w64(!in0 || !in1 || f0[1] != tag || f1[1] != tag) != 0) {
#pragma unroll 1
        for (unsigned spin = 0; spin < SPIN_CAP && __builtin_amdgcn_ballot_w64(f0[1] != tag || f1[1] != tag) != 0; spin++) {
          f0 = __builtin_amdgcn_raw_buffer_load_b64(rws, t0 * 8, WS_T, AUX_SC1);
          f1 = __builtin_amdgcn_raw_buffer_load_b64(rws, t1 * 8, WS_T, AUX_SC1);
        }
#pragma unroll 1
        for (int c = 0; c < 2; c++) {
          unsigned long long todo = __builtin_amdgcn_ballot_w64(c ? !in1 : !in0);
#pragma unroll 1
          while (todo) {
            const int l = __builtin_ctzll(todo);
            todo &= todo - 1;
            const unsigned ul = __builtin_amdgcn_readlane(u, l);
            const float h = c ? hi2f(ul) : lo2f(ul);
            f32x2 a = {0.f, 0.f};
#pragma unroll 1
            for (int j = 0; j < FF / 512; j++) {
              const u32x4 a1 = *(const u32x4*)(p.w1 + 512 * j + 8 * lane), a2 = *(const u32x4*)(p.b1 + 512 * j + 8 * lane), a3 = *(const u32x4*)(p.w2 + 512 * j + 8 * lane);
#pragma unroll
              for (int k = 0; k < 4; k++)
                a = term(f32x2{h, h}, f32x2{lo2f(a1[k]), hi2f(a1[k])} * KS, f32x2{lo2f(a2[k]), hi2f(a2[k])} * KS, f32x2{lo2f(a3[k]), hi2f(a3[k])} * (1.f / KS), a);
            }
            const float r = wave_sum(a[0] + a[1]);
            if (lane == l) { if (c) f1[0] = __float_as_uint(r); else f0[0] = __float_as_uint(r); }
          }
        }
      }
      const float b2 = lo2f(b2v);
      unsigned y = cvtpk(__uint_as_float(f0[0]), __uint_as_float(f1[0]));
      y = cvtpk(lo2f(y) + b2, hi2f(y) + b2);
      auto rout = __builtin_amdgcn_make_buffer_rsrc((void*)p.out, 0, 2 * DM * 2, 0x00020000);
      if (tid < 288) __builtin_amdgcn_raw_buffer_store_b32(cvtpk(lo2f(hh) + lo2f(y), hi2f(hh) + hi2f(y)), rout, tid * 4, fb * DM * 2, AUX_SC1);
      STAMP(11);
    }
  }
  STAMP(12);
  asm volatile(".Lfused_last:");
}

extern "C" void kernel_launch(void* const* d_in, const int* in_sizes, int n_in, void* d_out, int out_size, void* d_ws, size_t ws_size, hipStream_t stream) {
  Params p;
  p.x = (const u16*)d_in[0]; p.K = (const char*)d_in[1]; p.V = (const char*)d_in[2];
  p.wq = (const char*)d_in[3]; p.wo = (const char*)d_in[4];
  p.w1 = (const u16*)d_in[6]; p.b1 = (const u16*)d_in[7]; p.w2 = (const u16*)d_in[8]; p.b2 = (const u16*)d_in[9];
  p.out = (u16*)d_out; p.ws = (char*)d_ws;
  fused_kernel<<<256, 512, 0, stream>>>(p);
}
```

```cpp
#include <hip/hip_runtime.h>
#include <hip/hip_bf16.h>
#include <stdint.h>

#define DIAG 0
#define TOUCH0 1
#define RPERM 0
#define SLEEP 1
#define WOPOS 1
#define NPT 3
#define PLACE 0
#define XT 0
#define TK 0
#define TV 0
#define TN 2
#define WLATE 0
#define FIN0 254
#define BSTEP 8
#define PERM 21,16,9,10,8,20,17,18,14,19,13,15,12,22,11
#define BAR0 1

typedef unsigned u32x4 __attribute__((ext_vector_type(4)));
typedef unsigned u32x3 __attribute__((ext_vector_type(3)));
typedef unsigned u32x2 __attribute__((ext_vector_type(2)));
typedef float f32x4 __attribute__((ext_vector_type(4)));
typedef float f32x2 __attribute__((ext_vector_type(2)));
typedef __bf16 bf16x8 __attribute__((ext_vector_type(8)));
typedef __bf16 bf16x2 __attribute__((ext_vector_type(2)));
typedef short s16x4 __attribute__((ext_vector_type(4)));
typedef unsigned short u16;
#define LAS __attribute__((address_space(3)))

constexpr int DM = 576, FF = 4608;
constexpr float LOG2E = 1.4426950408889634f;
constexpr float SCALE = 0.08838834764831845f;
constexpr int NSL = 8;
constexpr int NTURN = 32 / NSL;
constexpr int NP = 8 * NPT;
constexpr int NTAB = 256 * NP;
constexpr unsigned TAB0 = (NPT == 3 ? 127 - 19 : 127 - 13) << 7;
constexpr unsigned SPIN_CAP = 1u << 16;
constexpr int AUX_NT = 2, AUX_SC1 = 16;

constexpr unsigned WS_EPOCH = 0;
constexpr unsigned WS_QG = 4096;
constexpr unsigned WS_P1 = WS_QG + 32 * 8 * 2048;
constexpr unsigned WS_ML = WS_P1 + 512 * 1024;
constexpr unsigned WS_P2 = WS_ML + 512 * 16;
constexpr unsigned WS_T = WS_P2 + 16 * DM * 8;
constexpr unsigned WS_END = WS_T + NTAB * 8;
constexpr unsigned WS_STAMP = 2u << 20;

constexpr int L_Q = 0;
constexpr int L_ML = 512;
constexpr int L_O = 640;
constexpr int L_OL = L_O + 8192;
constexpr int L_HS = L_OL + 2 * 1088;
constexpr int L_RED = L_HS + DM * 4;
constexpr int L_WO = L_RED + NP * 32;
constexpr int L_TAB = L_WO + 32768;
constexpr int L_END = L_TAB + NTAB * 8;

struct Params {
  const u16* x; const char* K; const char* V; const char* wq; const char* wo;
  const u16* w1; const u16* b1; const u16* w2; const u16* b2; u16* out; char* ws;
};

__device__ __forceinline__ float lo2f(unsigned v) { return __uint_as_float(v << 16); }
__device__ __forceinline__ float hi2f(unsigned v) { return __uint_as_float(v & 0xffff0000u); }
__device__ __forceinline__ unsigned cvtpk(float lo, float hi) {
  f32x2 v = {lo, hi};
  return __builtin_bit_cast(unsigned, __builtin_convertvector(v, bf16x2));
}
__device__ __forceinline__ float rbf(float x) { return lo2f(cvtpk(x, x)); }
template <int CTRL> __device__ __forceinline__ float dpp(float x) {
  return __builtin_bit_cast(float, __builtin_amdgcn_mov_dpp(__builtin_bit_cast(int, x), CTRL, 0xf, 0xf, true));
}
constexpr int XOR1 = 0xB1, XOR2 = 0x4E, HMIR = 0x141, MIR = 0x140;
__device__ __forceinline__ float row_sum(float x) {
  x += dpp<XOR1>(x); x += dpp<XOR2>(x); x += dpp<HMIR>(x); x += dpp<MIR>(x); return x;
}
__device__ __forceinline__ float x16_add(float a, float b) {
  auto s = __builtin_amdgcn_permlane16_swap(__float_as_uint(a), __float_as_uint(b), false, false);
  return __uint_as_float(s[0]) + __uint_as_float(s[1]);
}
__device__ __forceinline__ float x32_add(float a, float b) {
  auto s = __builtin_amdgcn_permlane32_swap(__float_as_uint(a), __float_as_uint(b), false, false);
  return __uint_as_float(s[0]) + __uint_as_float(s[1]);
}
__device__ __forceinline__ float x16_max(float x) {
  auto s = __builtin_amdgcn_permlane16_swap(__float_as_uint(x), __float_as_uint(x), false, false);
  return fmaxf(__uint_as_float(s[0]), __uint_as_float(s[1]));
}
__device__ __forceinline__ float x32_max(float x) {
  auto s = __builtin_amdgcn_permlane32_swap(__float_as_uint(x), __float_as_uint(x), false, false);
  return fmaxf(__uint_as_float(s[0]), __uint_as_float(s[1]));
}
__device__ __forceinline__ float wave_sum(float x) { x = row_sum(x); x = x16_add(x, x); return x32_add(x, x); }
__device__ __forceinline__ float rdlane(float x, int l) { return __builtin_bit_cast(float, __builtin_amdgcn_readlane(__builtin_bit_cast(int, x), l)); }
__device__ __forceinline__ float rden_of(float var) {
  return __builtin_amdgcn_rcpf(rbf(__builtin_amdgcn_sqrtf(rbf(rbf(var) + 1e-5f))));
}
__device__ __forceinline__ f32x4 mfma(u32x4 a, u32x4 b, f32x4 c) {
  return __builtin_amdgcn_mfma_f32_16x16x32_bf16(__builtin_bit_cast(bf16x8, a), __builtin_bit_cast(bf16x8, b), c, 0, 0, 0);
}
__device__ __forceinline__ u32x4 read_tr(LAS char* a, LAS char* b) {
  const u32x2 ua = __builtin_bit_cast(u32x2, __builtin_amdgcn_ds_read_tr16_b64_v4i16((LAS s16x4*)a));
  const u32x2 ub = __builtin_bit_cast(u32x2, __builtin_amdgcn_ds_read_tr16_b64_v4i16((LAS s16x4*)b));
  return u32x4{ua[0], ua[1], ub[0], ub[1]};
}
#define SB() __builtin_amdgcn_sched_barrier(0)
#define PIN(x) do { asm volatile("" : "+v"(x) :: "memory"); __builtin_amdgcn_sched_barrier(0); } while (0)
#define PINM() do { asm volatile("" ::: "memory"); __builtin_amdgcn_sched_barrier(0); } while (0)
#define SINK(x) asm volatile("" :: "v"(x))
#define VMWAIT(n) do { __builtin_amdgcn_sched_barrier(0); asm volatile("s_waitcnt vmcnt(" #n ")" ::: "memory"); __builtin_amdgcn_sched_barrier(0); } while (0)
#define LDS_BARRIER() do { asm volatile("s_waitcnt lgkmcnt(0)" ::: "memory"); __builtin_amdgcn_s_barrier(); asm volatile("" ::: "memory"); } while (0)
#define STAMP(i) do {} while (0)

constexpr float KS = 0.4686686930532063f;
constexpr float BETA = 2.f * LOG2E * 0.7978845608028654f / KS;
__device__ __forceinline__ f32x2 term(f32x2 h, f32x2 w1, f32x2 b1, f32x2 w2, f32x2 a) {
  const f32x2 v = __builtin_elementwise_fma(h, w1, b1);
  const f32x2 z = v * __builtin_elementwise_fma(v, v, f32x2{BETA, BETA});
  const f32x2 d = f32x2{__builtin_amdgcn_exp2f(-z[0]), __builtin_amdgcn_exp2f(-z[1])} + 1.f;
  return __builtin_elementwise_fma(v * w2, f32x2{__builtin_amdgcn_rcpf(d[0]), __builtin_amdgcn_rcpf(d[1])}, a);
}
template <int V> struct IC { static constexpr int value = V; };
template <int N, class F>
__device__ __forceinline__ void settle(u32x4 (&g)[N], unsigned tag, F load) {
#pragma unroll 1
  for (unsigned spin = 0; spin < SPIN_CAP; spin++) {
    unsigned y[N], any = 0;
#pragma unroll
    for (int s = 0; s < N; s++) { y[s] = __builtin_amdgcn_bitop3_b32(g[s][1], g[s][3], tag, 0x7e); any |= y[s]; }
    if (__builtin_amdgcn_ballot_w64(any != 0) == 0) break;
    __builtin_amdgcn_s_sleep(SLEEP);
    asm volatile("" ::: "memory");
#pragma unroll
    for (int s = 0; s < N; s++)
      if (__builtin_amdgcn_ballot_w64(y[s] != 0) != 0) g[s] = load(s);
  }
}
__device__ __forceinline__ float fadd(float a, float b) { float r; asm("v_add_f32 %0, %1, %2" : "=v"(r) : "v"(a), "v"(b)); return r; }
__device__ __forceinline__ float sum8(const u32x4 (&g)[8], int c) {
  float t[4];
#pragma unroll
  for (int i = 0; i < 4; i++) t[i] = fadd(__uint_as_float(g[2 * i][c]), __uint_as_float(g[2 * i + 1][c]));
  return fadd(fadd(t[0], t[1]), fadd(t[2], t[3]));
}

__global__ void __launch_bounds__(512, 2) fused_kernel(Params p) {
  __shared__ __attribute__((aligned(16))) char lds_[L_END];
  LAS char* const lds = (LAS char*)lds_;
  const int tid = threadIdx.x, lane = tid & 63;
  const int wave = __builtin_amdgcn_readfirstlane(tid >> 6);
  const int bid = blockIdx.x;
  const int g = lane >> 4, n = lane & 15;
  STAMP(0);
  const unsigned long long tstart = __builtin_amdgcn_s_memrealtime();
  auto rws = __builtin_amdgcn_make_buffer_rsrc((void*)p.ws, 0, WS_END, 0x00020000);
  auto rk = __builtin_amdgcn_make_buffer_rsrc((void*)p.K, 0, 256u << 20, 0x00020000);
  auto rv = __builtin_amdgcn_make_buffer_rsrc((void*)p.V, 0, 256u << 20, 0x00020000);
  char* pc; unsigned pclen;
  asm volatile("s_getpc_b64 %0\n.Lfused_first:\n\ts_mov_b32 %1, .Lfused_last-.Lfused_first" : "=s"(pc), "=s"(pclen));
  auto rc = __builtin_amdgcn_make_buffer_rsrc((void*)pc, 0, pclen, 0x00020000);
  unsigned epoch = *(const __attribute__((address_space(4))) unsigned*)(p.ws + WS_EPOCH);

  const int ab = bid >> 7, kvh = (bid >> 3) & 15, sp = bid & 7;
  constexpr int PB[15] = {PERM};
  auto sprd = [&](unsigned v, int first, int nb) { unsigned r = 0; for (int i = 0; i < nb; i++) r |= ((v >> i) & 1u) << PB[first + i]; return r; };
  unsigned va[4];
#pragma unroll
  for (int t = 0; t < 4; t++) va[t] = sprd(g + 4 * t, 0, 4) + n * 16;
  const unsigned sbase = ((unsigned)(bid >> 3) << 23) + sprd(wave, 4, 3);
  auto turn_off = [&](int i) { return sbase + sprd(i, 10, 2) + sprd(sp ^ (XT >> (4 * (i & 3))), 12, 3); };
  unsigned so = turn_off(0);
#define SO(s) (so + sprd(s, 7, 3))
  u32x4 rb[NSL][4];
#define LOAD(s, rs) _Pragma("unroll") for (int t = 0; t < 4; t++) rb[s][t] = __builtin_amdgcn_raw_buffer_load_b128(rs, va[t], SO(s), AUX_NT)

  unsigned msk[4];
  auto mkmask = [&]() {
    int nn = n;
    asm volatile("" : "+v"(nn));
#pragma unroll
    for (int k = 0; k < 4; k++) msk[k] = (((nn & 7) >> 1) == k) ? ((nn & 1) ? 0xffff0000u : 0x0000ffffu) : 0u;
  };

  unsigned tk[TN], tv[TN];
  auto pages = [&](const char* base, unsigned (&d)[TN]) {
    const char* a = base + (((unsigned)(bid >> 3) << 23) + (sprd(sp, 12, 3) & (1u << 22)));
#pragma unroll
    for (int i = 0; i < TN; i++) asm volatile("s_load_dword %0, %1, %2" : "=s"(d[i]) : "s"(a), "s"(i * ((4u << 20) / TN)) : "memory");
  };
  auto landed = [&](unsigned (&d)[TN]) {
    asm volatile("s_waitcnt lgkmcnt(0)" ::: "memory");
#pragma unroll
    for (int i = 0; i < TN; i++) asm volatile("" :: "s"(d[i]));
  };
  if (TK && wave == 1) pages(p.K, tk);
  if (TV == 1 && wave == 2) pages(p.V, tv);
  PINM();
  unsigned touch = 0;
  touch = __builtin_amdgcn_raw_buffer_load_b32(rc, lane * 64, ((wave + 7) & 7) * 4096, 0);
  PINM();
  if (wave == 0) {
    const int hq = bid >> 3, ks = bid & 7;
    auto rx = __builtin_amdgcn_make_buffer_rsrc((void*)p.x, 0, 2 * DM * 2, 0x00020000);
    auto rq = __builtin_amdgcn_make_buffer_rsrc((void*)p.wq, 0, DM * 4096 * 2, 0x00020000);
    u32x3 xs[3], xm[3];
    u32x4 wb[18];
#pragma unroll
    for (int i = 0; i < 3; i++) xs[i] = __builtin_amdgcn_raw_buffer_load_b96(rx, lane * 36 + 12 * i, 0, 0);
#pragma unroll
    for (int i = 0; i < 3; i++) xm[i] = __builtin_amdgcn_raw_buffer_load_b96(rx, ((n >> 3) * 32 + 4 * ks + g) * 36 + 12 * i, 0, 0);
#pragma unroll
    for (int t = 0; t < 18; t++) wb[t] = __builtin_amdgcn_raw_buffer_load_b128(rq, g * 18 * 8192 + n * 16, (72 * ks + t) * 8192 + hq * 256, AUX_NT);
    PINM();
    asm volatile("s_barrier" ::: "memory");
    PINM();
    mkmask();
    float s1 = 0.f;
#pragma unroll
    for (int i = 0; i < 9; i++) { const unsigned u = xs[i / 3][i % 3]; s1 += lo2f(u) + hi2f(u); }
    s1 = row_sum(s1);
    const float mean = x16_add(s1, s1) * (1.f / DM);
    float s2 = 0.f;
#pragma unroll
    for (int i = 0; i < 9; i++) { const unsigned u = xs[i / 3][i % 3]; const float a = lo2f(u) - mean, c = hi2f(u) - mean; s2 += a * a + c * c; }
    s2 = row_sum(s2);
    const float rden = rden_of(x16_add(s2, s2) * (1.f / DM)), mb = rbf(mean);
    const float mb0 = rdlane(mb, 0), mb1 = rdlane(mb, 32), rd0 = rdlane(rden, 0), rd1 = rdlane(rden, 32);
    const float mb_ = (n < 8) ? mb0 : mb1, rd_ = (n < 8) ? rd0 : rd1;
    f32x4 qa = {0, 0, 0, 0};
#pragma unroll
    for (int i = 0; i < 9; i++) {
      const unsigned u = xm[i / 3][i % 3], d = cvtpk(lo2f(u) - mb_, hi2f(u) - mb_);
      const float y0 = lo2f(d) * rd_, y1 = hi2f(d) * rd_;
      const unsigned p0 = cvtpk(y0, y0), p1 = cvtpk(y1, y1);
      qa = mfma(u32x4{p0 & msk[0], p0 & msk[1], p0 & msk[2], p0 & msk[3]}, wb[2 * i], qa);
      qa = mfma(u32x4{p1 & msk[0], p1 & msk[1], p1 & msk[2], p1 & msk[3]}, wb[2 * i + 1], qa);
    }
    unsigned tag = epoch;
    asm volatile("" : "+s"(tag));
    tag += 1;
    __builtin_amdgcn_raw_buffer_store_b128(u32x4{__float_as_uint(qa[0]), tag, __float_as_uint(qa[1]), tag}, rws, lane * 16, WS_QG + bid * 2048, AUX_SC1);
    __builtin_amdgcn_raw_buffer_store_b128(u32x4{__float_as_uint(qa[2]), tag, __float_as_uint(qa[3]), tag}, rws, lane * 16, WS_QG + bid * 2048 + 1024, AUX_SC1);
    STAMP(1);
  }
  else asm volatile("s_barrier" ::: "memory");
  PINM();
#pragma unroll
  for (int t = 0; t < 4; t++) PIN(va[t]);
#pragma unroll
  for (int s = 0; s < NSL; s++) { LOAD(s, rk); PINM(); }
  asm volatile("" : "+s"(epoch));
  const unsigned tag = epoch + 1;
  if (bid < 8 && tid == 0) __builtin_amdgcn_raw_buffer_store_b64(u32x2{(unsigned)tstart, tag}, rws, 0, 2048 + bid * 8, AUX_SC1);
  const bool fin = bid >= FIN0 && bid < FIN0 + 2;
  const int fb = bid & 1;
  u32x4 w1v, b1v, w2v; unsigned w1t, b1t, w2t, xh, b2v;
  auto ldw = [&]() {
    w1v = *(const u32x4*)(p.w1 + 8 * tid); b1v = *(const u32x4*)(p.b1 + 8 * tid); w2v = *(const u32x4*)(p.w2 + 8 * tid);
    w1t = p.w1[4096 + tid]; b1t = p.b1[4096 + tid]; w2t = p.w2[4096 + tid];
    xh = *(const unsigned*)(p.x + fb * DM + 2 * min(tid, 287)); b2v = p.b2[0];
    PINM();
  };
  if (!WLATE) ldw();
  if (TV == 2 && wave == 3) pages(p.V, tv);
  PINM();
  unsigned kw[4], kr[4];
#pragma unroll
  for (int t = 0; t < 4; t++) {
    kw[t] = L_WO + wave * 4096 + (4 * g + t) * 256 + ((n ^ (4 * g + t)) << 4);
    kr[t] = L_WO + wave * 4096 + n * 256 + (((4 * t + g) ^ n) << 4);
    PIN(kw[t]); PIN(kr[t]);
  }
  PINM();
  if (wave == 1 || wave == 2) {
    u32x4 pq[8];
    auto ldq = [&](int s) {
      return __builtin_amdgcn_raw_buffer_load_b128(rws, (lane >> 5) * 1024 + (32 * ab + (lane & 31)) * 16, WS_QG + ((2 * kvh + wave - 1) * 8 + s) * 2048, AUX_SC1);
    };
#pragma unroll
    for (int s = 0; s < 8; s++) pq[s] = ldq(s);
    STAMP(14);
    settle<8>(pq, tag, ldq);
    const float qa = sum8(pq, 0), qc = sum8(pq, 2);
    *(LAS unsigned*)(lds + L_Q + (wave - 1) * 256 + (8 * n + 4 * (g & 1) + 2 * (lane >> 5)) * 2) = cvtpk(qa, qc);
    STAMP(2);
  }
  SINK(touch);
  if (TK) landed(tk);
  if (TV) landed(tv);
  LDS_BARRIER();
  u32x4 qf[4];
#pragma unroll
  for (int s = 0; s < 4; s++) qf[s] = *(LAS u32x4*)(lds + L_Q + (n >> 3) * 256 + (32 * s + 8 * g) * 2);
  STAMP(3);

  unsigned Z[NTURN][2];
  f32x2 W1[4], B1[4], W2[4];
  float W1t, B1t, W2t, ya[4];
  auto point2 = [&](int k, unsigned t) {
    const unsigned hb = t >= NTAB / 2 ? t - NTAB / 2 + TAB0 + 0x8000u : t + TAB0;
    const float h0 = lo2f(hb), h1 = lo2f(hb + 1);
    const f32x2 tt = term(f32x2{h0, h1}, f32x2{W1t, W1t}, f32x2{B1t, B1t}, f32x2{W2t, W2t}, f32x2{0.f, 0.f});
    f32x2 a = {tt[0], 0.f}, c = {tt[1], 0.f};
#pragma unroll
    for (int j = 0; j < 4; j++) { a = term(f32x2{h0, h0}, W1[j], B1[j], W2[j], a); c = term(f32x2{h1, h1}, W1[j], B1[j], W2[j], c); }
    ya[k] = a[0] + a[1]; ya[k + 1] = c[0] + c[1];
    PIN(ya[k]); PIN(ya[k + 1]);
  };
  constexpr int PT = WLATE ? NP / 2 : 8, T0 = WLATE ? 2 : 1;
  auto kturn = [&](auto tab_, auto rs, int i) {
    constexpr bool tab = decltype(tab_)::value;
    const bool on = WLATE || NPT == NTURN - 1 || i <= NPT;
    unsigned z0 = 0, z1 = 0;
#pragma unroll
    for (int s = 0; s < NSL; s++) {
      if (s % BSTEP == 0) { SB(); __builtin_amdgcn_s_barrier(); SB(); }
#pragma unroll
      for (int t = 0; t < 4; t++) *(LAS u32x4*)(lds + kw[t]) = rb[s][t];
      PINM(); LOAD(s, rs); PINM();
      f32x4 sc = {0, 0, 0, 0};
#pragma unroll
      for (int t = 0; t < 4; t++) sc = mfma(*(LAS u32x4*)(lds + kr[t]), qf[t], sc);
      const unsigned u0 = cvtpk(sc[0], sc[1]), u1 = cvtpk(sc[2], sc[3]);
      z0 = (n & 7) == s ? u0 : z0; z1 = (n & 7) == s ? u1 : z1;
      PIN(z0); PIN(z1);
      if (tab && on) {
        const int k0 = (PLACE == 1 ? 0 : s * PT / 16) * 2, k1 = (PLACE == 1 ? (s == 7) * PT / 2 : (s + 1) * PT / 16) * 2;
#pragma unroll
        for (int k = k0; k < k1; k += 2) {
          point2(k & 3, bid * NP + PT * (i - T0) + k);
          if (k & 2) {
            const float r = row_sum(x16_add(x32_add(ya[0], ya[1]), x32_add(ya[2], ya[3])));
            if (n == 0) *(LAS float*)(lds + L_RED + (PT * (i - T0) + (k & ~3) + (g >> 1) + 2 * (g & 1)) * 32 + wave * 4) = r;
            PINM();
          }
        }
      }
    }
#pragma unroll
    for (int j = 0; j < NTURN - 1; j++) { Z[j][0] = Z[j + 1][0]; Z[j][1] = Z[j + 1][1]; }
    Z[NTURN - 1][0] = z0; Z[NTURN - 1][1] = z1;
  };
#pragma unroll 1
  for (int i = 0; i < T0; i++) {
    so = turn_off(i + 1);
    if (WLATE && i == T0 - 1) ldw();
    kturn(IC<0>{}, rk, i);
  }
#pragma unroll
  for (int j = 0; j < 4; j++) {
    W1[j] = f32x2{lo2f(w1v[j]), hi2f(w1v[j])} * KS;
    B1[j] = f32x2{lo2f(b1v[j]), hi2f(b1v[j])} * KS;
    W2[j] = f32x2{lo2f(w2v[j]), hi2f(w2v[j])} * (1.f / KS);
    PIN(W1[j]); PIN(B1[j]); PIN(W2[j]);
  }
  W1t = lo2f(w1t) * KS; B1t = lo2f(b1t) * KS; W2t = lo2f(w2t) * (1.f / KS);
  PIN(W1t); PIN(B1t); PIN(W2t);
#pragma unroll 1
  for (int i = T0; i < NTURN; i++) {
    const bool last = i == NTURN - 1;
    so = turn_off(i + 1);
    kturn(IC<1>{}, __builtin_amdgcn_make_buffer_rsrc((void*)(last ? p.V : p.K), 0, 256u << 20, 0x00020000), i);
  }
  STAMP(15);
  unsigned sel[2][4];
  {
    float z[4 * NTURN];
#pragma unroll
    for (int j = 0; j < 2 * NTURN; j++) {
      const unsigned u = Z[j >> 1][j & 1], w = cvtpk(lo2f(u) * SCALE, hi2f(u) * SCALE);
      z[2 * j] = lo2f(w); z[2 * j + 1] = hi2f(w);
    }
    float mx = z[0];
#pragma unroll
    for (int j = 1; j < 4 * NTURN; j++) mx = fmaxf(mx, z[j]);
    mx = fmaxf(mx, dpp<XOR1>(mx)); mx = fmaxf(mx, dpp<XOR2>(mx)); mx = fmaxf(mx, dpp<HMIR>(mx));
    mx = x32_max(x16_max(mx));
    if (lane == 0 || lane == 8) *(LAS float*)(lds + L_ML + (n >> 3) * 32 + wave * 4) = mx;
#pragma unroll
    for (int k = 0; k < 4; k++) {
      const bool on = ((n & 7) >> 1) == k;
      sel[0][k] = on ? ((n & 1) ? 0x01000c0cu : 0x0c0c0100u) : 0x0c0c0c0cu;
      sel[1][k] = on ? ((n & 1) ? 0x03020c0cu : 0x0c0c0302u) : 0x0c0c0c0cu;
    }
    LDS_BARRIER();
    if (wave == 1 && lane < NP) {
      const f32x4 a = *(LAS f32x4*)(lds + L_RED + lane * 32), c = *(LAS f32x4*)(lds + L_RED + lane * 32 + 16);
      __builtin_amdgcn_raw_buffer_store_b64(u32x2{__float_as_uint(((a[0] + a[1]) + (a[2] + a[3])) + ((c[0] + c[1]) + (c[2] + c[3]))), tag}, rws, lane * 8, WS_T + bid * NP * 8, AUX_SC1);
    }
    PINM();
    const f32x4 ma = *(LAS f32x4*)(lds + L_ML + (n >> 3) * 32), mb = *(LAS f32x4*)(lds + L_ML + (n >> 3) * 32 + 16);
    const float m2 = fmaxf(fmaxf(fmaxf(ma[0], ma[1]), fmaxf(ma[2], ma[3])), fmaxf(fmaxf(mb[0], mb[1]), fmaxf(mb[2], mb[3]))) * LOG2E;
    float l = 0.f;
#pragma unroll
    for (int j = 0; j < 2 * NTURN; j++) {
      const float ea = __builtin_amdgcn_exp2f(fmaf(z[2 * j], LOG2E, -m2)), eb = __builtin_amdgcn_exp2f(fmaf(z[2 * j + 1], LOG2E, -m2));
      l += ea + eb;
      Z[j >> 1][j & 1] = cvtpk(ea, eb);
      asm volatile("" : "+v"(Z[j >> 1][j & 1]));
    }
    l += dpp<XOR1>(l); l += dpp<XOR2>(l); l += dpp<HMIR>(l);
    l = x16_add(l, l); l = x32_add(l, l);
    if (lane == 0 || lane == 8) *(LAS float*)(lds + L_ML + 64 + (n >> 3) * 32 + wave * 4) = l;
    LDS_BARRIER();
    if (wave == 0) {
      const f32x4 la = *(LAS f32x4*)(lds + L_ML + 64 + (n >> 3) * 32), lb = *(LAS f32x4*)(lds + L_ML + 64 + (n >> 3) * 32 + 16);
      const float L = ((la[0] + la[1]) + (la[2] + la[3])) + ((lb[0] + lb[1]) + (lb[2] + lb[3]));
      if (lane == 0 || lane == 8)
        __builtin_amdgcn_raw_buffer_store_b128(u32x4{__float_as_uint(m2), tag, __float_as_uint(L), tag}, rws, (n >> 3) * 128, WS_ML + ((ab * 32 + 2 * kvh) * 8 + sp) * 16, AUX_SC1);
    }
  }
  const int rho = bid & 7, cg = bid >> 3;
  auto ldwo = [&](int row0, int cnt) {
    const u32x4 ro = {(unsigned)(uintptr_t)p.wo, (unsigned)((uintptr_t)p.wo >> 32) & 0xffffu, 4096 * DM * 2, 0x00020000};
    const unsigned vo = (lane >> 2) * 1152 + (((lane & 3) ^ ((lane >> 4) & 2)) << 4);
#pragma unroll
    for (int t = 0; t < cnt; t++)
      asm volatile("s_mov_b32 m0, %0\n\ts_nop 0\n\tbuffer_load_dwordx4 %1, %2, %3 offen nt lds"
                   :: "s"((unsigned)(uintptr_t)lds + L_WO + (row0 + 16 * t) * 64), "v"(vo), "s"(ro), "s"((512 * rho + row0 + 16 * t) * 1152 + 64 * cg) : "memory");
  };
  PINM();
  f32x4 acc = {0, 0, 0, 0};
  auto pv = [&](auto sc_, const unsigned (&P)[2], const u32x4 (&v)[4]) {
    constexpr int s = decltype(sc_)::value;
#pragma unroll
    for (int h = 0; h < 2; h++) {
      int b = __builtin_amdgcn_mov_dpp((int)P[h], 0x150 + s, 0xf, 0xf, true);
      b = __builtin_amdgcn_update_dpp(b, (int)P[h], 0x158 + s, 0xf, 0xc, false);
#pragma unroll
      for (int c = 0; c < 2; c++)
        acc = mfma(u32x4{__builtin_amdgcn_perm(b, b, sel[c][0]), __builtin_amdgcn_perm(b, b, sel[c][1]), __builtin_amdgcn_perm(b, b, sel[c][2]), __builtin_amdgcn_perm(b, b, sel[c][3])}, v[2 * h + c], acc);
    }
  };
  u32x4 gm[1];
  auto rml = __builtin_amdgcn_make_buffer_rsrc((void*)p.ws, 0, wave < 2 ? WS_END : 0, 0x00020000);
  auto ldm = [&](int) { return __builtin_amdgcn_raw_buffer_load_b128(rml, (lane & 7) * 16, WS_ML + (ab * 32 + 2 * kvh + wave) * 128, AUX_SC1); };
#define FOR8(X) X(0) X(1) X(2) X(3) X(4) X(5) X(6) X(7)
#pragma unroll 1
  for (int i = 0; i < NTURN - 1; i++) {
    so = turn_off(i + 1);
    gm[0] = ldm(0);
    if (fin && i == NTURN - 2) {
      const u32x4 rt = {(unsigned)(uintptr_t)p.ws, (unsigned)((uintptr_t)p.ws >> 32) & 0xffffu, WS_END, 0x00020000};
#pragma unroll
      for (int t = 0; t < NP / 4; t++)
        asm volatile("s_mov_b32 m0, %0\n\ts_nop 0\n\tbuffer_load_dwordx4 %1, %2, %3 offen sc1 lds"
                     :: "s"((unsigned)(uintptr_t)lds + L_TAB + (wave * (NP / 4) + t) * 1024), "v"(lane * 16), "s"(rt), "s"(WS_T + (wave * (NP / 4) + t) * 1024) : "memory");
    }
    PINM();
#define VSTEP(s) if (s % BSTEP == 0) { SB(); __builtin_amdgcn_s_barrier(); SB(); } pv(IC<s>{}, Z[0], rb[s]); PIN(acc); LOAD(s, rv); SB();
    FOR8(VSTEP)
#pragma unroll
    for (int j = 0; j < NTURN - 1; j++) { Z[j][0] = Z[j + 1][0]; Z[j][1] = Z[j + 1][1]; }
  }
  STAMP(4);
  const unsigned hrow = (wave >> 2) * 32 + 4 * rho + (wave & 3);
  auto ld1 = [&](int s) { return __builtin_amdgcn_raw_buffer_load_b128(rws, lane * 16, WS_P1 + hrow * 8192 + s * 1024, AUX_SC1); };
  u32x4 go[8], bfr[16];
  float wgt = 0.f;
  {
    __builtin_amdgcn_s_barrier();
#define VLAST(s) pv(IC<s>{}, Z[0], rb[s]); SB();
    FOR8(VLAST)
    PIN(acc);
    if (wave < 2) {
      settle<1>(gm, tag, ldm);
      const float ms = __uint_as_float(gm[0][0]);
      float M = fmaxf(ms, dpp<XOR1>(ms)); M = fmaxf(M, dpp<XOR2>(M)); M = fmaxf(M, dpp<HMIR>(M));
      const float e = __builtin_amdgcn_exp2f(ms - M);
      float L = e * __uint_as_float(gm[0][2]); L += dpp<XOR1>(L); L += dpp<XOR2>(L); L += dpp<HMIR>(L);
      wgt = rdlane(e * __builtin_amdgcn_rcpf(L), sp);
    }
    PINM();
    if (wave < 4 && cg < 18) ldwo(128 * wave, 8);
    SB();
  }
  STAMP(5);
  *(LAS f32x4*)(lds + L_O + wave * 1024 + (g >> 1) * 512 + (8 * n + 4 * (g & 1)) * 4) = acc;
  LDS_BARRIER();
  STAMP(13);
  if (wave < 2) {
    f32x2 o[8];
#pragma unroll
    for (int w = 0; w < 8; w++) o[w] = *(LAS f32x2*)(lds + L_O + w * 1024 + wave * 512 + lane * 8);
    const f32x2 t = (((o[0] + o[1]) + (o[2] + o[3])) + ((o[4] + o[5]) + (o[6] + o[7]))) * wgt;
    __builtin_amdgcn_raw_buffer_store_b128(u32x4{__float_as_uint(t[0]), tag, __float_as_uint(t[1]), tag}, rws, lane * 16, WS_P1 + ((ab * 32 + 2 * kvh + wave) * 8 + sp) * 1024, AUX_SC1);
  }
  STAMP(6);
  if (cg < 18) {
#pragma unroll
    for (int s = 0; s < 8; s++) go[s] = ld1(s);
    unsigned olw = L_OL + (wave >> 2) * 1088 + (wave & 3) * 256 + lane * 4, olr = L_OL + (n & 1) * 1088 + 16 * g, p2o = (g * 8 * DM + n) * 8;
    PIN(olw); PIN(olr); PIN(p2o);
    VMWAIT(8);
    __builtin_amdgcn_s_barrier();
    PINM();
    if (wave < 2) {
#pragma unroll
      for (int s = 0; s < 16; s++) {
        const int a = L_WO + (32 * s + 8 * g + (n >> 2)) * 64 + ((wave ^ (g & 1)) << 5) + (n & 3) * 8;
        bfr[s] = read_tr(lds + a, lds + a + 256);
      }
    }
    settle<8>(go, tag, ld1);
    STAMP(7);
    const float o0 = sum8(go, 0), o1 = sum8(go, 2);
    *(LAS unsigned*)(lds + olw) = cvtpk(o0, o1);
    LDS_BARRIER();
    if (wave < 2) {
      u32x4 af[16];
#pragma unroll
      for (int s = 0; s < 16; s++) af[s] = *(LAS u32x4*)(lds + olr + 64 * s);
      SB();
      f32x4 d[4] = {};
#pragma unroll
      for (int s = 0; s < 16; s++) d[s & 3] = mfma(af[s], bfr[s], d[s & 3]);
      const float r = g ? (d[0][1] + d[1][1]) + (d[2][1] + d[3][1]) : (d[0][0] + d[1][0]) + (d[2][0] + d[3][0]);
      if (g < 2) __builtin_amdgcn_raw_buffer_store_b64(u32x2{__float_as_uint(r), tag}, rws, p2o, WS_P2 + (rho * DM + 32 * cg + 16 * wave) * 8, AUX_SC1);
    }
    STAMP(8);
  }
  PINM();
  if (fin) {
    unsigned hh = 0;
    if (wave < 5) {
      u32x4 gp[8];
      auto ld2 = [&](int s) { return __builtin_amdgcn_raw_buffer_load_b128(rws, min(tid, 287) * 16, WS_P2 + (fb * 8 + s) * DM * 8, AUX_SC1); };
#pragma unroll
      for (int s = 0; s < 8; s++) gp[s] = ld2(s);
      settle<8>(gp, tag, ld2);
      const unsigned at = cvtpk(sum8(gp, 0), sum8(gp, 2));
      hh = cvtpk(lo2f(xh) + lo2f(at), hi2f(xh) + hi2f(at));
      if (tid < 288) *(LAS f32x2*)(lds + L_HS + tid * 8) = f32x2{lo2f(hh), hi2f(hh)};
    }
    STAMP(9);
    LDS_BARRIER();
    if (wave == 7) {
      if (fb == 0 && lane == 0) __builtin_amdgcn_raw_buffer_store_b32(tag, rws, 0, WS_EPOCH, AUX_SC1);
    } else if (wave < 5) {
      float v[9];
#pragma unroll
      for (int i = 0; i < 9; i++) v[i] = *(LAS float*)(lds + L_HS + (lane + 64 * i) * 4);
      const float mean = wave_sum(((v[0] + v[1]) + (v[2] + v[3])) + ((v[4] + v[5]) + (v[6] + v[7])) + v[8]) * (1.f / DM);
      float s2a = 0.f, s2b = 0.f;
#pragma unroll
      for (int i = 0; i < 9; i++) { const float a = v[i] - mean; if (i & 1) s2b = fmaf(a, a, s2b); else s2a = fmaf(a, a, s2a); }
      const float var = wave_sum(s2a + s2b) * (1.f / DM);
      const float rden = rden_of(var), mb = rbf(mean);
      const float mb2 = rbf((mean - mb) * rden), rden2 = rden_of(var * rden * rden);
      unsigned u = cvtpk(lo2f(hh) - mb, hi2f(hh) - mb);
      u = cvtpk(lo2f(u) * rden, hi2f(u) * rden);
      u = cvtpk(lo2f(u) - mb2, hi2f(u) - mb2);
      u = cvtpk(lo2f(u) * rden2, hi2f(u) * rden2);
      STAMP(10);
      const unsigned m0 = (u & 0x7fffu) - TAB0, m1 = ((u >> 16) & 0x7fffu) - TAB0;
      const bool in0 = m0 < NTAB / 2, in1 = m1 < NTAB / 2;
      const unsigned t0 = in0 ? m0 + (u >> 15 & 1) * (NTAB / 2) : 0, t1 = in1 ? m1 + (u >> 31) * (NTAB / 2) : 0;
      u32x2 f0 = *(LAS u32x2*)(lds + L_TAB + t0 * 8), f1 = *(LAS u32x2*)(lds + L_TAB + t1 * 8);
      if (__builtin_amdgcn_ballot_w64(!in0 || !in1 || f0[1] != tag || f1[1] != tag) != 0) {
#pragma unroll 1
        for (unsigned spin = 0; spin < SPIN_CAP && __builtin_amdgcn_ballot_w64(f0[1] != tag || f1[1] != tag) != 0; spin++) {
          f0 = __builtin_amdgcn_raw_buffer_load_b64(rws, t0 * 8, WS_T, AUX_SC1);
          f1 = __builtin_amdgcn_raw_buffer_load_b64(rws, t1 * 8, WS_T, AUX_SC1);
        }
#pragma unroll 1
        for (int c = 0; c < 2; c++) {
          unsigned long long todo = __builtin_amdgcn_ballot_w64(c ? !in1 : !in0);
#pragma unroll 1
          while (todo) {
            const int l = __builtin_ctzll(todo);
            todo &= todo - 1;
            const unsigned ul = __builtin_amdgcn_readlane(u, l);
            const float h = c ? hi2f(ul) : lo2f(ul);
            f32x2 a = {0.f, 0.f};
#pragma unroll 1
            for (int j = 0; j < FF / 512; j++) {
              const u32x4 a1 = *(const u32x4*)(p.w1 + 512 * j + 8 * lane), a2 = *(const u32x4*)(p.b1 + 512 * j + 8 * lane), a3 = *(const u32x4*)(p.w2 + 512 * j + 8 * lane);
#pragma unroll
              for (int k = 0; k < 4; k++)
                a = term(f32x2{h, h}, f32x2{lo2f(a1[k]), hi2f(a1[k])} * KS, f32x2{lo2f(a2[k]), hi2f(a2[k])} * KS, f32x2{lo2f(a3[k]), hi2f(a3[k])} * (1.f / KS), a);
            }
            const float r = wave_sum(a[0] + a[1]);
            if (lane == l) { if (c) f1[0] = __float_as_uint(r); else f0[0] = __float_as_uint(r); }
          }
        }
      }
      { u32x2 w = __builtin_amdgcn_raw_buffer_load_b64(rws, (lane & 7) * 8, 2048, AUX_SC1);
        unsigned el = (unsigned)tstart - w[0]; el = (int)el < 0 ? 0 : el;
        float e = (float)el; e = fmaxf(e, dpp<XOR1>(e)); e = fmaxf(e, dpp<XOR2>(e)); e = fmaxf(e, dpp<HMIR>(e));
        const unsigned back = __builtin_amdgcn_readfirstlane((unsigned)e);
        while (__builtin_amdgcn_s_memrealtime() - tstart + back < 10000) __builtin_amdgcn_s_sleep(1); }
      const float b2 = lo2f(b2v);
      unsigned y = cvtpk(__uint_as_float(f0[0]), __uint_as_float(f1[0]));
      y = cvtpk(lo2f(y) + b2, hi2f(y) + b2);
      auto rout = __builtin_amdgcn_make_buffer_rsrc((void*)p.out, 0, 2 * DM * 2, 0x00020000);
      if (tid < 288) __builtin_amdgcn_raw_buffer_store_b32(cvtpk(lo2f(hh) + lo2f(y), hi2f(hh) + hi2f(y)), rout, tid * 4, fb * DM * 2, AUX_SC1);
      STAMP(11);
    }
  }
  STAMP(12);
  asm volatile(".Lfused_last:");
}

extern "C" void kernel_launch(void* const* d_in, const int* in_sizes, int n_in, void* d_out, int out_size, void* d_ws, size_t ws_size, hipStream_t stream) {
  Params p;
  p.x = (const u16*)d_in[0]; p.K = (const char*)d_in[1]; p.V = (const char*)d_in[2];
  p.wq = (const char*)d_in[3]; p.wo = (const char*)d_in[4];
  p.w1 = (const u16*)d_in[6]; p.b1 = (const u16*)d_in[7]; p.w2 = (const u16*)d_in[8]; p.b2 = (const u16*)d_in[9];
  p.out = (u16*)d_out; p.ws = (char*)d_ws;
  fused_kernel<<<256, 512, 0, stream>>>(p);
}
```

```cpp
#include <hip/hip_runtime.h>
#include <hip/hip_bf16.h>
#include <stdint.h>

#define DIAG 0
#define RPERM 0
#define SLEEP 0
#define NPT 3
#define BQ 10
#define TTOP 1
#define FINE 1
#define PRI 0
#define OWK 63
#define OWV 63
#define VMW2(n) VMWAIT(n)
#define PLACE 5
#define BK 0x55
#define BV 0x55
#define BL 1
#define NOTAB 0
#define NOQW 0
#define LAY 6
#define FIN0 254
#define BSTEP 8
#define PM ongbimcahlejfkd
#define STR2(x) #x
#define STR(x) STR2(x)
#define PMB(i) (STR(PM)[i] - 'a' + 8)
#define PERM PMB(0), PMB(1), PMB(2), PMB(3), PMB(4), PMB(5), PMB(6), PMB(7), PMB(8), PMB(9), PMB(10), PMB(11), PMB(12), PMB(13), PMB(14)

typedef unsigned u32x4 __attribute__((ext_vector_type(4)));
typedef unsigned u32x3 __attribute__((ext_vector_type(3)));
typedef unsigned u32x2 __attribute__((ext_vector_type(2)));
typedef float f32x4 __attribute__((ext_vector_type(4)));
typedef float f32x2 __attribute__((ext_vector_type(2)));
typedef __bf16 bf16x8 __attribute__((ext_vector_type(8)));
typedef __bf16 bf16x2 __attribute__((ext_vector_type(2)));
typedef short s16x4 __attribute__((ext_vector_type(4)));
typedef unsigned short u16;
#define LAS __attribute__((address_space(3)))

constexpr int DM = 576, FF = 4608;
constexpr float LOG2E = 1.4426950408889634f;
constexpr float SCALE = 0.08838834764831845f;
constexpr int NSL = 8;
constexpr int NTURN = 32 / NSL;
constexpr int NP = 8 * NPT;
constexpr int NTAB = 256 * NP;
constexpr unsigned TAB0 = (NPT == 3 ? 127 - 19 : 127 - 13) << 7;
constexpr unsigned SPIN_CAP = 1u << 16;
constexpr int AUX_NT = 2, AUX_SC1 = 16;

constexpr unsigned WS_EPOCH = 0;
constexpr unsigned WS_QG = 4096;
constexpr unsigned WS_P1 = WS_QG + 32 * 8 * 2048;
constexpr unsigned WS_ML = WS_P1 + 512 * 1024;
constexpr unsigned WS_P2 = WS_ML + 512 * 16;
constexpr unsigned WS_T = WS_P2 + 16 * DM * 8;
constexpr unsigned WS_END = WS_T + NTAB * 8;
constexpr unsigned WS_STAMP = 2u << 20;

constexpr int L_Q = 0;
constexpr int L_ML = 512;
constexpr int L_O = 640;
constexpr int L_OL = L_O + 8192;
constexpr int L_HS = L_OL + 2 * 1088;
constexpr int L_RED = L_HS + DM * 4;
constexpr int L_WO = L_RED + NP * 32;
constexpr int L_TAB = L_WO + 32768;
constexpr int L_END = L_TAB + NTAB * 8;

struct Params {
  const u16* x; const char* K; const char* V; const char* wq; const char* wo;
  const u16* w1; const u16* b1; const u16* w2; const u16* b2; u16* out; char* ws;
};

__device__ __forceinline__ float lo2f(unsigned v) { return __uint_as_float(v << 16); }
__device__ __forceinline__ float hi2f(unsigned v) { return __uint_as_float(v & 0xffff0000u); }
__device__ __forceinline__ unsigned cvtpk(float lo, float hi) {
  f32x2 v = {lo, hi};
  return __builtin_bit_cast(unsigned, __builtin_convertvector(v, bf16x2));
}
__device__ __forceinline__ float rbf(float x) { return lo2f(cvtpk(x, x)); }
template <int CTRL> __device__ __forceinline__ float dpp(float x) {
  return __builtin_bit_cast(float, __builtin_amdgcn_mov_dpp(__builtin_bit_cast(int, x), CTRL, 0xf, 0xf, true));
}
constexpr int XOR1 = 0xB1, XOR2 = 0x4E, HMIR = 0x141, MIR = 0x140;
__device__ __forceinline__ float row_sum(float x) {
  x += dpp<XOR1>(x); x += dpp<XOR2>(x); x += dpp<HMIR>(x); x += dpp<MIR>(x); return x;
}
__device__ __forceinline__ float x16_add(float a, float b) {
  auto s = __builtin_amdgcn_permlane16_swap(__float_as_uint(a), __float_as_uint(b), false, false);
  return __uint_as_float(s[0]) + __uint_as_float(s[1]);
}
__device__ __forceinline__ float x32_add(float a, float b) {
  auto s = __builtin_amdgcn_permlane32_swap(__float_as_uint(a), __float_as_uint(b), false, false);
  return __uint_as_float(s[0]) + __uint_as_float(s[1]);
}
__device__ __forceinline__ float x16_max(float x) {
  auto s = __builtin_amdgcn_permlane16_swap(__float_as_uint(x), __float_as_uint(x), false, false);
  return fmaxf(__uint_as_float(s[0]), __uint_as_float(s[1]));
}
__device__ __forceinline__ float x32_max(float x) {
  auto s = __builtin_amdgcn_permlane32_swap(__float_as_uint(x), __float_as_uint(x), false, false);
  return fmaxf(__uint_as_float(s[0]), __uint_as_float(s[1]));
}
__device__ __forceinline__ float wave_sum(float x) { x = row_sum(x); x = x16_add(x, x); return x32_add(x, x); }
__device__ __forceinline__ float rdlane(float x, int l) { return __builtin_bit_cast(float, __builtin_amdgcn_readlane(__builtin_bit_cast(int, x), l)); }
__device__ __forceinline__ float rden_of(float var) {
  return __builtin_amdgcn_rcpf(rbf(__builtin_amdgcn_sqrtf(rbf(rbf(var) + 1e-5f))));
}
__device__ __forceinline__ f32x4 mfma(u32x4 a, u32x4 b, f32x4 c) {
  return __builtin_amdgcn_mfma_f32_16x16x32_bf16(__builtin_bit_cast(bf16x8, a), __builtin_bit_cast(bf16x8, b), c, 0, 0, 0);
}
__device__ __forceinline__ u32x4 read_tr(LAS char* a, LAS char* b) {
  const u32x2 ua = __builtin_bit_cast(u32x2, __builtin_amdgcn_ds_read_tr16_b64_v4i16((LAS s16x4*)a));
  const u32x2 ub = __builtin_bit_cast(u32x2, __builtin_amdgcn_ds_read_tr16_b64_v4i16((LAS s16x4*)b));
  return u32x4{ua[0], ua[1], ub[0], ub[1]};
}
#define SB() __builtin_amdgcn_sched_barrier(0)
#define PIN(x) do { asm volatile("" : "+v"(x) :: "memory"); __builtin_amdgcn_sched_barrier(0); } while (0)
#define PINM() do { asm volatile("" ::: "memory"); __builtin_amdgcn_sched_barrier(0); } while (0)
#define SINK(x) asm volatile("" :: "v"(x))
#define VMWAIT(n) do { __builtin_amdgcn_sched_barrier(0); asm volatile("s_waitcnt vmcnt(" #n ")" ::: "memory"); __builtin_amdgcn_sched_barrier(0); } while (0)
#define LDS_BARRIER() do { asm volatile("s_waitcnt lgkmcnt(0)" ::: "memory"); __builtin_amdgcn_s_barrier(); asm volatile("" ::: "memory"); } while (0)
#define STAMP(i) do {} while (0)

constexpr float KS = 0.4686686930532063f;
constexpr float BETA = 2.f * LOG2E * 0.7978845608028654f / KS;
__device__ __forceinline__ f32x2 term(f32x2 h, f32x2 w1, f32x2 b1, f32x2 w2, f32x2 a) {
  const f32x2 v = __builtin_elementwise_fma(h, w1, b1);
  const f32x2 z = v * __builtin_elementwise_fma(v, v, f32x2{BETA, BETA});
  const f32x2 d = f32x2{__builtin_amdgcn_exp2f(-z[0]), __builtin_amdgcn_exp2f(-z[1])} + 1.f;
  return __builtin_elementwise_fma(v * w2, f32x2{__builtin_amdgcn_rcpf(d[0]), __builtin_amdgcn_rcpf(d[1])}, a);
}
template <int V> struct IC { static constexpr int value = V; };
template <int N, class F>
__device__ __forceinline__ void settle(u32x4 (&g)[N], unsigned tag, F load) {
#pragma unroll 1
  for (unsigned spin = 0; spin < SPIN_CAP; spin++) {
    unsigned y[N], any = 0;
#pragma unroll
    for (int s = 0; s < N; s++) { y[s] = __builtin_amdgcn_bitop3_b32(g[s][1], g[s][3], tag, 0x7e); any |= y[s]; }
    if (__builtin_amdgcn_ballot_w64(any != 0) == 0) break;
    asm volatile("" ::: "memory");
#pragma unroll
    for (int s = 0; s < N; s++)
      if (__builtin_amdgcn_ballot_w64(y[s] != 0) != 0) g[s] = load(s);
  }
}
__device__ __forceinline__ float fadd(float a, float b) { float r; asm("v_add_f32 %0, %1, %2" : "=v"(r) : "v"(a), "v"(b)); return r; }
__device__ __forceinline__ float sum8(const u32x4 (&g)[8], int c) {
  float t[4];
#pragma unroll
  for (int i = 0; i < 4; i++) t[i] = fadd(__uint_as_float(g[2 * i][c]), __uint_as_float(g[2 * i + 1][c]));
  return fadd(fadd(t[0], t[1]), fadd(t[2], t[3]));
}

__global__ void __launch_bounds__(512, 2) fused_kernel(Params p) {
  __shared__ __attribute__((aligned(16))) char lds_[L_END];
  LAS char* const lds = (LAS char*)lds_;
  const int tid = threadIdx.x, lane = tid & 63;
  const int wave = __builtin_amdgcn_readfirstlane(tid >> 6);
  const int bid = blockIdx.x;
  const int g = lane >> 4, n = lane & 15;
  STAMP(0);
  char* pc; unsigned pclen;
  asm volatile("s_getpc_b64 %0\n.Lfused_first:\n\ts_mov_b32 %1, .Lfused_last-.Lfused_first" : "=s"(pc), "=s"(pclen));
  auto rc = __builtin_amdgcn_make_buffer_rsrc((void*)pc, 0, pclen, 0x00020000);
  unsigned touch = 0;
  if (TTOP) { touch = __builtin_amdgcn_raw_buffer_load_b32(rc, tid * 64, 0, 0); PINM(); }

  const int ab = bid >> 7, kvh = (bid >> 3) & 15, sp = bid & 7;
  constexpr int PB[15] = {PERM};
  auto sprd = [&](unsigned v, int first, int nb) { unsigned r = 0; for (int i = 0; i < nb; i++) r |= ((v >> i) & 1u) << PB[first + i]; return r; };
  unsigned va[4];
#pragma unroll
  for (int t = 0; t < 4; t++) va[t] = sprd(g + 4 * t, 0, 4) + n * 16;
  const unsigned sbase = ((unsigned)(bid >> 3) << 23) + sprd(wave, 4, 3);
  auto turn_off = [&](int i) { return sbase + sprd(i, 10, 2) + sprd(sp, 12, 3); };
  unsigned so = turn_off(0);
#define SO(s) (so + sprd(s, 7, 3))
  u32x4 rb[NSL][4];
#define LOAD(s, rs) _Pragma("unroll") for (int t = 0; t < 4; t++) rb[s][t] = __builtin_amdgcn_raw_buffer_load_b128(rs, va[t], SO(s), AUX_NT)

  unsigned msk[4];
  auto mkmask = [&]() {
    int nn = n;
    asm volatile("" : "+v"(nn));
#pragma unroll
    for (int k = 0; k < 4; k++) msk[k] = (((nn & 7) >> 1) == k) ? ((nn & 1) ? 0xffff0000u : 0x0000ffffu) : 0u;
  };

  if (LAY >= 4) PINM();
  if (!TTOP) touch = __builtin_amdgcn_raw_buffer_load_b32(rc, lane * 64, ((wave + 7) & 7) * 4096, 0);
  PINM();
  u32x3 xs[3], xm[3];
  u32x4 wb[18];
#pragma unroll
  for (int t = 0; t < 4; t++) PIN(va[t]);
  asm volatile("" : "+s"(so));
  PINM();
  const char* pk = p.K;
  asm volatile("" : "+s"(pk));
  auto rws = __builtin_amdgcn_make_buffer_rsrc((void*)p.ws, 0, WS_END, 0x00020000);
  auto rk = __builtin_amdgcn_make_buffer_rsrc((void*)pk, 0, 256u << 20, 0x00020000);
  auto rv = __builtin_amdgcn_make_buffer_rsrc((void*)p.V, 0, 256u << 20, 0x00020000);
  unsigned epoch = *(const __attribute__((address_space(4))) unsigned*)(p.ws + WS_EPOCH);
  if (LAY == 1 ? __builtin_expect(wave == 0, 0) : wave == 0) {
    const int hq = bid >> 3, ks = bid & 7;
    auto rx = __builtin_amdgcn_make_buffer_rsrc((void*)p.x, 0, 2 * DM * 2, 0x00020000);
    auto rq = __builtin_amdgcn_make_buffer_rsrc((void*)p.wq, 0, DM * 4096 * 2, 0x00020000);
#pragma unroll
    for (int i = 0; i < 3; i++) xs[i] = __builtin_amdgcn_raw_buffer_load_b96(rx, lane * 36 + 12 * i, 0, 0);
#pragma unroll
    for (int i = 0; i < 3; i++) xm[i] = __builtin_amdgcn_raw_buffer_load_b96(rx, ((n >> 3) * 32 + 4 * ks + g) * 36 + 12 * i, 0, 0);
#pragma unroll
    for (int t = 0; t < 18; t++) {
      if (t == BQ) { PINM(); asm volatile("s_barrier" ::: "memory"); PINM(); }
      wb[t] = __builtin_amdgcn_raw_buffer_load_b128(rq, g * 18 * 8192 + n * 16, (72 * ks + t) * 8192 + hq * 256, AUX_NT);
    }
    PINM();
    if (BQ == 18) asm volatile("s_barrier" ::: "memory");
    PINM();
    int one = 1;
    if (LAY == 6) asm volatile("" : "+s"(one));
    if (__builtin_expect(one, 0)) {
    mkmask();
    float s1 = 0.f;
#pragma unroll
    for (int i = 0; i < 9; i++) { const unsigned u = xs[i / 3][i % 3]; s1 += lo2f(u) + hi2f(u); }
    s1 = row_sum(s1);
    const float mean = x16_add(s1, s1) * (1.f / DM);
    float s2 = 0.f;
#pragma unroll
    for (int i = 0; i < 9; i++) { const unsigned u = xs[i / 3][i % 3]; const float a = lo2f(u) - mean, c = hi2f(u) - mean; s2 += a * a + c * c; }
    s2 = row_sum(s2);
    const float rden = rden_of(x16_add(s2, s2) * (1.f / DM)), mb = rbf(mean);
    const float mb0 = rdlane(mb, 0), mb1 = rdlane(mb, 32), rd0 = rdlane(rden, 0), rd1 = rdlane(rden, 32);
    const float mb_ = (n < 8) ? mb0 : mb1, rd_ = (n < 8) ? rd0 : rd1;
    f32x4 qa = {0, 0, 0, 0};
#pragma unroll
    for (int i = 0; i < 9; i++) {
      const unsigned u = xm[i / 3][i % 3], d = cvtpk(lo2f(u) - mb_, hi2f(u) - mb_);
      const float y0 = lo2f(d) * rd_, y1 = hi2f(d) * rd_;
      const unsigned p0 = cvtpk(y0, y0), p1 = cvtpk(y1, y1);
      qa = mfma(u32x4{p0 & msk[0], p0 & msk[1], p0 & msk[2], p0 & msk[3]}, wb[2 * i], qa);
      qa = mfma(u32x4{p1 & msk[0], p1 & msk[1], p1 & msk[2], p1 & msk[3]}, wb[2 * i + 1], qa);
    }
    unsigned tag = epoch;
    asm volatile("" : "+s"(tag));
    tag += 1;
    __builtin_amdgcn_raw_buffer_store_b128(u32x4{__float_as_uint(qa[0]), tag, __float_as_uint(qa[1]), tag}, rws, lane * 16, WS_QG + bid * 2048, AUX_SC1);
    __builtin_amdgcn_raw_buffer_store_b128(u32x4{__float_as_uint(qa[2]), tag, __float_as_uint(qa[3]), tag}, rws, lane * 16, WS_QG + bid * 2048 + 1024, AUX_SC1);
    STAMP(1);
    } else __builtin_amdgcn_s_waitcnt(0x0f70);
  }
  else asm volatile("s_barrier" ::: "memory");
  PINM();
#pragma unroll
  for (int t = 0; t < 4; t++) PIN(va[t]);
#pragma unroll
  for (int s = 0; s < NSL; s++) { LOAD(s, rk); PINM(); }
  asm volatile("" : "+s"(epoch));
  const unsigned tag = epoch + 1;
  const bool fin = bid >= FIN0 && bid < FIN0 + 2;
  const int fb = bid & 1;
  u32x4 w1v, b1v, w2v; unsigned w1t, b1t, w2t, xh, b2v;
  auto ldw = [&]() {
    if (NOTAB == 3 || NOTAB == 1) { w1v = b1v = w2v = u32x4{tag, tag, tag, tag}; w1t = b1t = w2t = tag; asm volatile("" : "+v"(w1v), "+v"(b1v), "+v"(w2v), "+v"(w1t), "+v"(b1t), "+v"(w2t)); } else {
    w1v = *(const u32x4*)(p.w1 + 8 * tid); b1v = *(const u32x4*)(p.b1 + 8 * tid); w2v = *(const u32x4*)(p.w2 + 8 * tid);
    w1t = p.w1[4096 + tid]; b1t = p.b1[4096 + tid]; w2t = p.w2[4096 + tid]; }
    xh = *(const unsigned*)(p.x + fb * DM + 2 * min(tid, 287)); b2v = p.b2[0];
    PINM();
  };
  ldw();
  unsigned kw[4], kr[4];
#pragma unroll
  for (int t = 0; t < 4; t++) {
    kw[t] = L_WO + wave * 4096 + (4 * g + t) * 256 + ((n ^ (4 * g + t)) << 4);
    kr[t] = L_WO + wave * 4096 + n * 256 + (((4 * t + g) ^ n) << 4);
    PIN(kw[t]); PIN(kr[t]);
  }
  PINM();
  if (wave == 1 || wave == 2) {
    u32x4 pq[8];
    auto ldq = [&](int s) {
      return __builtin_amdgcn_raw_buffer_load_b128(rws, (lane >> 5) * 1024 + (32 * ab + (lane & 31)) * 16, WS_QG + ((2 * kvh + wave - 1) * 8 + s) * 2048, AUX_SC1);
    };
#pragma unroll
    for (int s = 0; s < 8; s++) pq[s] = ldq(s);
    STAMP(14);
    settle<8>(pq, tag, ldq);
    const float qa = sum8(pq, 0), qc = sum8(pq, 2);
    *(LAS unsigned*)(lds + L_Q + (wave - 1) * 256 + (8 * n + 4 * (g & 1) + 2 * (lane >> 5)) * 2) = cvtpk(qa, qc);
    STAMP(2);
  }
  SINK(touch);
  LDS_BARRIER();
  u32x4 qf[4];
#pragma unroll
  for (int s = 0; s < 4; s++) qf[s] = *(LAS u32x4*)(lds + L_Q + (n >> 3) * 256 + (32 * s + 8 * g) * 2);
  STAMP(3);

  unsigned Z[NTURN][2];
  f32x2 W1[4], B1[4], W2[4];
  float W1t, B1t, W2t, ya[4];
  auto point1 = [&](int k, unsigned t) {
    const unsigned hb = t >= NTAB / 2 ? t - NTAB / 2 + TAB0 + 0x8000u : t + TAB0;
    const float h0 = lo2f(hb), h1 = lo2f(hb + 1);
    if (!(k & 1)) {
      const f32x2 tt = term(f32x2{h0, h1}, f32x2{W1t, W1t}, f32x2{B1t, B1t}, f32x2{W2t, W2t}, f32x2{0.f, 0.f});
      ya[k] = tt[0]; ya[k + 1] = tt[1];
      PIN(ya[k + 1]);
    }
    f32x2 a = {ya[k], 0.f};
#pragma unroll
    for (int j = 0; j < 4; j++) a = term(f32x2{h0, h0}, W1[j], B1[j], W2[j], a);
    ya[k] = a[0] + a[1];
    PIN(ya[k]);
  };
  constexpr int PT = 8, T0 = 1;
  auto kturn = [&](auto tab_, auto rs, int i) {
    constexpr bool tab = decltype(tab_)::value;
    const bool on = NPT == NTURN - 1 || i <= NPT;
    unsigned z0 = 0, z1 = 0;
    auto points = [&](int k0, int k1) {
#pragma unroll
      for (int k = k0; k < k1; k++) {
        point1(k & 3, bid * NP + PT * (i - T0) + k);
        if ((k & 3) == 3) {
          const float r = row_sum(x16_add(x32_add(ya[0], ya[1]), x32_add(ya[2], ya[3])));
          if (n == 0) *(LAS float*)(lds + L_RED + (PT * (i - T0) + (k & ~3) + (g >> 1) + 2 * (g & 1)) * 32 + wave * 4) = r;
          PINM();
        }
      }
    };
#pragma unroll
    for (int s = 0; s < NSL; s++) {
      if ((BK >> s & 1) && (tab || s)) { SB(); __builtin_amdgcn_s_barrier(); SB(); }
      if (tab && on && PLACE == 1 && s == 0) points(0, 8);
      if (OWK < 63 && tab) VMW2(OWK);
#pragma unroll
      for (int t = 0; t < 4; t++) *(LAS u32x4*)(lds + kw[t]) = rb[s][t];
      PINM(); LOAD(s, rs); PINM();
      f32x4 sc = {0, 0, 0, 0};
#pragma unroll
      for (int t = 0; t < 4; t++) sc = mfma(*(LAS u32x4*)(lds + kr[t]), qf[t], sc);
      const unsigned u0 = cvtpk(sc[0], sc[1]), u1 = cvtpk(sc[2], sc[3]);
      z0 = (n & 7) == s ? u0 : z0; z1 = (n & 7) == s ? u1 : z1;
      PIN(z0); PIN(z1);
      if (tab && NOTAB >= 4) { SB(); __builtin_amdgcn_s_sleep(NOTAB); SB(); }
      if (tab && on && (NOTAB == 0 || NOTAB == 3)) {
        if (PLACE == 0) points(s, s + 1);
        if (PLACE == 2 && s == 7) points(0, 8);
        if (PLACE == 3 && s == 3) points(0, 8);
        if (PLACE == 4 && (s & 3) == 3) points(s - 3, s + 1);
        if (PLACE == 5) { constexpr int c[9] = {0, 1, 2, 3, 5, 6, 7, 8, 8}; points(c[s], c[s + 1]); }
        if (PLACE == 6) { constexpr int c[9] = {0, 2, 4, 6, 8, 8, 8, 8, 8}; points(c[s], c[s + 1]); }
        if (PLACE == 7) { constexpr int c[9] = {0, 1, 3, 4, 6, 7, 8, 8, 8}; points(c[s], c[s + 1]); }
      }
    }
#pragma unroll
    for (int j = 0; j < NTURN - 1; j++) { Z[j][0] = Z[j + 1][0]; Z[j][1] = Z[j + 1][1]; }
    Z[NTURN - 1][0] = z0; Z[NTURN - 1][1] = z1;
  };
  so = turn_off(1);
  if (PRI) __builtin_amdgcn_s_setprio(1);
  kturn(IC<0>{}, rk, 0);
#pragma unroll
  for (int j = 0; j < 4; j++) {
    W1[j] = f32x2{lo2f(w1v[j]), hi2f(w1v[j])} * KS;
    B1[j] = f32x2{lo2f(b1v[j]), hi2f(b1v[j])} * KS;
    W2[j] = f32x2{lo2f(w2v[j]), hi2f(w2v[j])} * (1.f / KS);
    PIN(W1[j]); PIN(B1[j]); PIN(W2[j]);
  }
  W1t = lo2f(w1t) * KS; B1t = lo2f(b1t) * KS; W2t = lo2f(w2t) * (1.f / KS);
  PIN(W1t); PIN(B1t); PIN(W2t);
#pragma unroll 1
  for (int i = T0; i < NTURN; i++) {
    const bool last = i == NTURN - 1;
    so = turn_off(i + 1);
    kturn(IC<1>{}, __builtin_amdgcn_make_buffer_rsrc((void*)(last ? p.V : p.K), 0, 256u << 20, 0x00020000), i);
  }
  STAMP(15);
  unsigned sel[2][4];
  {
    float z[4 * NTURN];
#pragma unroll
    for (int j = 0; j < 2 * NTURN; j++) {
      const unsigned u = Z[j >> 1][j & 1], w = cvtpk(lo2f(u) * SCALE, hi2f(u) * SCALE);
      z[2 * j] = lo2f(w); z[2 * j + 1] = hi2f(w);
    }
    float mx = z[0];
#pragma unroll
    for (int j = 1; j < 4 * NTURN; j++) mx = fmaxf(mx, z[j]);
    mx = fmaxf(mx, dpp<XOR1>(mx)); mx = fmaxf(mx, dpp<XOR2>(mx)); mx = fmaxf(mx, dpp<HMIR>(mx));
    mx = x32_max(x16_max(mx));
    if (lane == 0 || lane == 8) *(LAS float*)(lds + L_ML + (n >> 3) * 32 + wave * 4) = mx;
#pragma unroll
    for (int k = 0; k < 4; k++) {
      const bool on = ((n & 7) >> 1) == k;
      sel[0][k] = on ? ((n & 1) ? 0x01000c0cu : 0x0c0c0100u) : 0x0c0c0c0cu;
      sel[1][k] = on ? ((n & 1) ? 0x03020c0cu : 0x0c0c0302u) : 0x0c0c0c0cu;
    }
    LDS_BARRIER();
    if (wave == 1 && lane < NP) {
      const f32x4 a = *(LAS f32x4*)(lds + L_RED + lane * 32), c = *(LAS f32x4*)(lds + L_RED + lane * 32 + 16);
      __builtin_amdgcn_raw_buffer_store_b64(u32x2{__float_as_uint(((a[0] + a[1]) + (a[2] + a[3])) + ((c[0] + c[1]) + (c[2] + c[3]))), tag}, rws, lane * 8, WS_T + bid * NP * 8, AUX_SC1);
    }
    PINM();
    const f32x4 ma = *(LAS f32x4*)(lds + L_ML + (n >> 3) * 32), mb = *(LAS f32x4*)(lds + L_ML + (n >> 3) * 32 + 16);
    const float m2 = fmaxf(fmaxf(fmaxf(ma[0], ma[1]), fmaxf(ma[2], ma[3])), fmaxf(fmaxf(mb[0], mb[1]), fmaxf(mb[2], mb[3]))) * LOG2E;
    float l = 0.f;
#pragma unroll
    for (int j = 0; j < 2 * NTURN; j++) {
      const float ea = __builtin_amdgcn_exp2f(fmaf(z[2 * j], LOG2E, -m2)), eb = __builtin_amdgcn_exp2f(fmaf(z[2 * j + 1], LOG2E, -m2));
      l += ea + eb;
      Z[j >> 1][j & 1] = cvtpk(ea, eb);
      asm volatile("" : "+v"(Z[j >> 1][j & 1]));
    }
    l += dpp<XOR1>(l); l += dpp<XOR2>(l); l += dpp<HMIR>(l);
    l = x16_add(l, l); l = x32_add(l, l);
    if (lane == 0 || lane == 8) *(LAS float*)(lds + L_ML + 64 + (n >> 3) * 32 + wave * 4) = l;
    LDS_BARRIER();
    if (wave == 0) {
      const f32x4 la = *(LAS f32x4*)(lds + L_ML + 64 + (n >> 3) * 32), lb = *(LAS f32x4*)(lds + L_ML + 64 + (n >> 3) * 32 + 16);
      const float L = ((la[0] + la[1]) + (la[2] + la[3])) + ((lb[0] + lb[1]) + (lb[2] + lb[3]));
      if (lane == 0 || lane == 8)
        __builtin_amdgcn_raw_buffer_store_b128(u32x4{__float_as_uint(m2), tag, __float_as_uint(L), tag}, rws, (n >> 3) * 128, WS_ML + ((ab * 32 + 2 * kvh) * 8 + sp) * 16, AUX_SC1);
    }
  }
  const int rho = bid & 7, cg = bid >> 3;
  auto ldwo = [&](int row0, int cnt) {
    const u32x4 ro = {(unsigned)(uintptr_t)p.wo, (unsigned)((uintptr_t)p.wo >> 32) & 0xffffu, 4096 * DM * 2, 0x00020000};
    const unsigned vo = (lane >> 2) * 1152 + (((lane & 3) ^ ((lane >> 4) & 2)) << 4);
#pragma unroll
    for (int t = 0; t < cnt; t++)
      asm volatile("s_mov_b32 m0, %0\n\ts_nop 0\n\tbuffer_load_dwordx4 %1, %2, %3 offen nt lds"
                   :: "s"((unsigned)(uintptr_t)lds + L_WO + (row0 + 16 * t) * 64), "v"(vo), "s"(ro), "s"((512 * rho + row0 + 16 * t) * 1152 + 64 * cg) : "memory");
  };
  PINM();
  f32x4 acc = {0, 0, 0, 0};
  auto pv = [&](auto sc_, const unsigned (&P)[2], const u32x4 (&v)[4]) {
    constexpr int s = decltype(sc_)::value;
#pragma unroll
    for (int h = 0; h < 2; h++) {
      int b = __builtin_amdgcn_mov_dpp((int)P[h], 0x150 + s, 0xf, 0xf, true);
      b = __builtin_amdgcn_update_dpp(b, (int)P[h], 0x158 + s, 0xf, 0xc, false);
#pragma unroll
      for (int c = 0; c < 2; c++)
        acc = mfma(u32x4{__builtin_amdgcn_perm(b, b, sel[c][0]), __builtin_amdgcn_perm(b, b, sel[c][1]), __builtin_amdgcn_perm(b, b, sel[c][2]), __builtin_amdgcn_perm(b, b, sel[c][3])}, v[2 * h + c], acc);
    }
  };
  u32x4 gm[1];
  auto rml = __builtin_amdgcn_make_buffer_rsrc((void*)p.ws, 0, wave < 2 ? WS_END : 0, 0x00020000);
  auto ldm = [&](int) { return __builtin_amdgcn_raw_buffer_load_b128(rml, (lane & 7) * 16, WS_ML + (ab * 32 + 2 * kvh + wave) * 128, AUX_SC1); };
#define FOR8(X) X(0) X(1) X(2) X(3) X(4) X(5) X(6) X(7)
#pragma unroll 1
  for (int i = 0; i < NTURN - 1; i++) {
    so = turn_off(i + 1);
    gm[0] = ldm(0);
    if (fin && i == NTURN - 2) {
      const u32x4 rt = {(unsigned)(uintptr_t)p.ws, (unsigned)((uintptr_t)p.ws >> 32) & 0xffffu, WS_END, 0x00020000};
#pragma unroll
      for (int t = 0; t < NP / 4; t++)
        asm volatile("s_mov_b32 m0, %0\n\ts_nop 0\n\tbuffer_load_dwordx4 %1, %2, %3 offen sc1 lds"
                     :: "s"((unsigned)(uintptr_t)lds + L_TAB + (wave * (NP / 4) + t) * 1024), "v"(lane * 16), "s"(rt), "s"(WS_T + (wave * (NP / 4) + t) * 1024) : "memory");
    }
    PINM();
#define VSTEP(s) if (BV >> s & 1) { SB(); __builtin_amdgcn_s_barrier(); SB(); } if (OWV < 63) VMW2(OWV); pv(IC<s>{}, Z[0], rb[s]); PIN(acc); LOAD(s, rv); SB();
    FOR8(VSTEP)
#pragma unroll
    for (int j = 0; j < NTURN - 1; j++) { Z[j][0] = Z[j + 1][0]; Z[j][1] = Z[j + 1][1]; }
  }
  STAMP(4);
  const unsigned hrow = (wave >> 2) * 32 + 4 * rho + (wave & 3);
  auto ld1 = [&](int s) { return __builtin_amdgcn_raw_buffer_load_b128(rws, lane * 16, WS_P1 + hrow * 8192 + s * 1024, AUX_SC1); };
  u32x4 go[8], bfr[16];
  float wgt = 0.f;
  {
    if (BL) __builtin_amdgcn_s_barrier();
    if (wave < 2) {
      if (__builtin_amdgcn_ballot_w64(__builtin_amdgcn_bitop3_b32(gm[0][1], gm[0][3], tag, 0x7e) != 0) != 0) { settle<1>(gm, tag, ldm); __builtin_amdgcn_s_waitcnt(0x0f70); }
      const float ms = __uint_as_float(gm[0][0]);
      float M = fmaxf(ms, dpp<XOR1>(ms)); M = fmaxf(M, dpp<XOR2>(M)); M = fmaxf(M, dpp<HMIR>(M));
      const float e = __builtin_amdgcn_exp2f(ms - M);
      float L = e * __uint_as_float(gm[0][2]); L += dpp<XOR1>(L); L += dpp<XOR2>(L); L += dpp<HMIR>(L);
      wgt = rdlane(e * __builtin_amdgcn_rcpf(L), sp);
    }
    PINM();
#define VLAST(s) pv(IC<s>{}, Z[0], rb[s]); SB();
    FOR8(VLAST)
    PIN(acc);
    PINM();
    if (wave < 4 && cg < 18) ldwo(128 * wave, 8);
    SB();
  }
  STAMP(5);
  *(LAS f32x4*)(lds + L_O + wave * 1024 + (g >> 1) * 512 + (8 * n + 4 * (g & 1)) * 4) = acc;
  LDS_BARRIER();
  STAMP(13);
  if (wave < 2) {
    f32x2 o[8];
#pragma unroll
    for (int w = 0; w < 8; w++) o[w] = *(LAS f32x2*)(lds + L_O + w * 1024 + wave * 512 + lane * 8);
    const f32x2 t = (((o[0] + o[1]) + (o[2] + o[3])) + ((o[4] + o[5]) + (o[6] + o[7]))) * wgt;
    __builtin_amdgcn_raw_buffer_store_b128(u32x4{__float_as_uint(t[0]), tag, __float_as_uint(t[1]), tag}, rws, lane * 16, WS_P1 + ((ab * 32 + 2 * kvh + wave) * 8 + sp) * 1024, AUX_SC1);
  }
  STAMP(6);
  if (cg < 18) {
    unsigned olw = L_OL + (wave >> 2) * 1088 + (wave & 3) * 256 + lane * 4, olr = L_OL + (n & 1) * 1088 + 16 * g, p2o = (g * 8 * DM + n) * 8;
#pragma unroll
    for (int s = 0; s < 8; s++) go[s] = ld1(s);
    PIN(olw); PIN(olr); PIN(p2o);
    VMWAIT(8);
    __builtin_amdgcn_s_barrier();
    PINM();
    if (wave < 2) {
#pragma unroll
      for (int s = 0; s < 16; s++) {
        const int a = L_WO + (32 * s + 8 * g + (n >> 2)) * 64 + ((wave ^ (g & 1)) << 5) + (n & 3) * 8;
        bfr[s] = read_tr(lds + a, lds + a + 256);
      }
    }
    settle<8>(go, tag, ld1);
    STAMP(7);
    const float o0 = sum8(go, 0), o1 = sum8(go, 2);
    *(LAS unsigned*)(lds + olw) = cvtpk(o0, o1);
    LDS_BARRIER();
    if (wave < 2) {
      u32x4 af[16];
#pragma unroll
      for (int s = 0; s < 16; s++) af[s] = *(LAS u32x4*)(lds + olr + 64 * s);
      SB();
      f32x4 d[4] = {};
#pragma unroll
      for (int s = 0; s < 16; s++) d[s & 3] = mfma(af[s], bfr[s], d[s & 3]);
      const float r = g ? (d[0][1] + d[1][1]) + (d[2][1] + d[3][1]) : (d[0][0] + d[1][0]) + (d[2][0] + d[3][0]);
      if (g < 2) __builtin_amdgcn_raw_buffer_store_b64(u32x2{__float_as_uint(r), tag}, rws, p2o, WS_P2 + (rho * DM + 32 * cg + 16 * wave) * 8, AUX_SC1);
    }
    STAMP(8);
  }
  PINM();
  if (fin) {
    unsigned hh = 0;
    if (wave < 5) {
      u32x4 gp[8];
      auto ld2 = [&](int s) { return __builtin_amdgcn_raw_buffer_load_b128(rws, min(tid, 287) * 16, WS_P2 + (fb * 8 + s) * DM * 8, AUX_SC1); };
#pragma unroll
      for (int s = 0; s < 8; s++) gp[s] = ld2(s);
      settle<8>(gp, tag, ld2);
      const unsigned at = cvtpk(sum8(gp, 0), sum8(gp, 2));
      hh = cvtpk(lo2f(xh) + lo2f(at), hi2f(xh) + hi2f(at));
      if (tid < 288) *(LAS f32x2*)(lds + L_HS + tid * 8) = f32x2{lo2f(hh), hi2f(hh)};
    }
    STAMP(9);
    LDS_BARRIER();
    if (wave == 7) {
      if (fb == 0 && lane == 0) __builtin_amdgcn_raw_buffer_store_b32(tag, rws, 0, WS_EPOCH, AUX_SC1);
    } else if (wave < 5) {
      float v[9];
#pragma unroll
      for (int i = 0; i < 9; i++) v[i] = *(LAS float*)(lds + L_HS + (lane + 64 * i) * 4);
      const float mean = wave_sum(((v[0] + v[1]) + (v[2] + v[3])) + ((v[4] + v[5]) + (v[6] + v[7])) + v[8]) * (1.f / DM);
      float s2a = 0.f, s2b = 0.f;
#pragma unroll
      for (int i = 0; i < 9; i++) { const float a = v[i] - mean; if (i & 1) s2b = fmaf(a, a, s2b); else s2a = fmaf(a, a, s2a); }
      const float var = wave_sum(s2a + s2b) * (1.f / DM);
      const float rden = rden_of(var), mb = rbf(mean);
      const float mb2 = rbf((mean - mb) * rden), rden2 = rden_of(var * rden * rden);
      unsigned u = cvtpk(lo2f(hh) - mb, hi2f(hh) - mb);
      u = cvtpk(lo2f(u) * rden, hi2f(u) * rden);
      u = cvtpk(lo2f(u) - mb2, hi2f(u) - mb2);
      u = cvtpk(lo2f(u) * rden2, hi2f(u) * rden2);
      STAMP(10);
      const unsigned m0 = (u & 0x7fffu) - TAB0, m1 = ((u >> 16) & 0x7fffu) - TAB0;
      const bool in0 = m0 < NTAB / 2, in1 = m1 < NTAB / 2;
      const unsigned t0 = in0 ? m0 + (u >> 15 & 1) * (NTAB / 2) : 0, t1 = in1 ? m1 + (u >> 31) * (NTAB / 2) : 0;
      u32x2 f0 = *(LAS u32x2*)(lds + L_TAB + t0 * 8), f1 = *(LAS u32x2*)(lds + L_TAB + t1 * 8);
      if (__builtin_amdgcn_ballot_w64(!in0 || !in1 || f0[1] != tag || f1[1] != tag) != 0) {
#pragma unroll 1
        for (unsigned spin = 0; spin < SPIN_CAP && __builtin_amdgcn_ballot_w64(f0[1] != tag || f1[1] != tag) != 0; spin++) {
          f0 = __builtin_amdgcn_raw_buffer_load_b64(rws, t0 * 8, WS_T, AUX_SC1);
          f1 = __builtin_amdgcn_raw_buffer_load_b64(rws, t1 * 8, WS_T, AUX_SC1);
        }
#pragma unroll 1
        for (int c = 0; c < 2; c++) {
          unsigned long long todo = __builtin_amdgcn_ballot_w64(c ? !in1 : !in0);
#pragma unroll 1
          while (todo) {
            const int l = __builtin_ctzll(todo);
            todo &= todo - 1;
            const unsigned ul = __builtin_amdgcn_readlane(u, l);
            const float h = c ? hi2f(ul) : lo2f(ul);
            f32x2 a = {0.f, 0.f};
#pragma unroll 1
            for (int j = 0; j < FF / 512; j++) {
              const u32x4 a1 = *(const u32x4*)(p.w1 + 512 * j + 8 * lane), a2 = *(const u32x4*)(p.b1 + 512 * j + 8 * lane), a3 = *(const u32x4*)(p.w2 + 512 * j + 8 * lane);
#pragma unroll
              for (int k = 0; k < 4; k++)
                a = term(f32x2{h, h}, f32x2{lo2f(a1[k]), hi2f(a1[k])} * KS, f32x2{lo2f(a2[k]), hi2f(a2[k])} * KS, f32x2{lo2f(a3[k]), hi2f(a3[k])} * (1.f / KS), a);
            }
            const float r = wave_sum(a[0] + a[1]);
            if (lane == l) { if (c) f1[0] = __float_as_uint(r); else f0[0] = __float_as_uint(r); }
          }
        }
      }
      const float b2 = lo2f(b2v);
      unsigned y = cvtpk(__uint_as_float(f0[0]), __uint_as_float(f1[0]));
      y = cvtpk(lo2f(y) + b2, hi2f(y) + b2);
      auto rout = __builtin_amdgcn_make_buffer_rsrc((void*)p.out, 0, 2 * DM * 2, 0x00020000);
      if (tid < 288) __builtin_amdgcn_raw_buffer_store_b32(cvtpk(lo2f(hh) + lo2f(y), hi2f(hh) + hi2f(y)), rout, tid * 4, fb * DM * 2, AUX_SC1);
      STAMP(11);
    }
  }
  STAMP(12);
  asm volatile(".Lfused_last:");
}

extern "C" void kernel_launch(void* const* d_in, const int* in_sizes, int n_in, void* d_out, int out_size, void* d_ws, size_t ws_size, hipStream_t stream) {
  Params p;
  p.x = (const u16*)d_in[0]; p.K = (const char*)d_in[1]; p.V = (const char*)d_in[2];
  p.wq = (const char*)d_in[3]; p.wo = (const char*)d_in[4];
  p.w1 = (const u16*)d_in[6]; p.b1 = (const u16*)d_in[7]; p.w2 = (const u16*)d_in[8]; p.b2 = (const u16*)d_in[9];
  p.out = (u16*)d_out; p.ws = (char*)d_ws;
  fused_kernel<<<256, 512, 0, stream>>>(p);
}
```

```cpp
#include <hip/hip_runtime.h>
#include <hip/hip_bf16.h>
#include <stdint.h>

typedef unsigned u32x4 __attribute__((ext_vector_type(4)));
typedef unsigned u32x3 __attribute__((ext_vector_type(3)));
typedef unsigned u32x2 __attribute__((ext_vector_type(2)));
typedef float f32x4 __attribute__((ext_vector_type(4)));
typedef float f32x2 __attribute__((ext_vector_type(2)));
typedef __bf16 bf16x8 __attribute__((ext_vector_type(8)));
typedef __bf16 bf16x2 __attribute__((ext_vector_type(2)));
typedef short s16x4 __attribute__((ext_vector_type(4)));
typedef unsigned short u16;
#define LAS __attribute__((address_space(3)))

constexpr int DM = 576, FF = 4608;
constexpr float LOG2E = 1.4426950408889634f;
constexpr float SCALE = 0.08838834764831845f;
constexpr int NSL = 8;
constexpr int NTURN = 32 / NSL;
constexpr int NP = 24;
constexpr int NTAB = 256 * NP;
constexpr unsigned TAB0 = (127 - 19) << 7;
constexpr unsigned SPIN_CAP = 1u << 16;
constexpr int AUX_NT = 2, AUX_SC1 = 16;
constexpr int PB[15] = {12, 21, 8, 14, 11, 15, 10, 22, 19, 20, 17, 18, 9, 16, 13};
constexpr int T_A = 970, T_STEP = 1714, T_SPAN = 214, PACER = 7;

constexpr unsigned WS_EPOCH = 0;
constexpr unsigned WS_QG = 4096;
constexpr unsigned WS_P1 = WS_QG + 32 * 8 * 2048;
constexpr unsigned WS_ML = WS_P1 + 512 * 1024;
constexpr unsigned WS_P2 = WS_ML + 512 * 16;
constexpr unsigned WS_T = WS_P2 + 16 * DM * 8;
constexpr unsigned WS_M = WS_T + NTAB * 8;
constexpr unsigned WS_END = WS_M + 14 * 512;

constexpr int L_Q = 0;
constexpr int L_ML = 512;
constexpr int L_O = 640;
constexpr int L_OL = L_O + 8192;
constexpr int L_HS = L_OL + 2 * 1088;
constexpr int L_RED = L_HS + DM * 4;
constexpr int L_WO = L_RED + NP * 32;
constexpr int L_TAB = L_WO + 32768;
constexpr int L_END = L_TAB + NTAB * 8;

struct Params {
  const u16* x; const char* K; const char* V; const char* wq; const char* wo;
  const u16* w1; const u16* b1; const u16* w2; const u16* b2; u16* out; char* ws;
};

__device__ __forceinline__ float lo2f(unsigned v) { return __uint_as_float(v << 16); }
__device__ __forceinline__ float hi2f(unsigned v) { return __uint_as_float(v & 0xffff0000u); }
__device__ __forceinline__ unsigned cvtpk(float lo, float hi) {
  f32x2 v = {lo, hi};
  return __builtin_bit_cast(unsigned, __builtin_convertvector(v, bf16x2));
}
__device__ __forceinline__ float rbf(float x) { return lo2f(cvtpk(x, x)); }
template <int CTRL> __device__ __forceinline__ float dpp(float x) {
  return __builtin_bit_cast(float, __builtin_amdgcn_mov_dpp(__builtin_bit_cast(int, x), CTRL, 0xf, 0xf, true));
}
constexpr int XOR1 = 0xB1, XOR2 = 0x4E, HMIR = 0x141, MIR = 0x140;
__device__ __forceinline__ float row_sum(float x) {
  x += dpp<XOR1>(x); x += dpp<XOR2>(x); x += dpp<HMIR>(x); x += dpp<MIR>(x); return x;
}
__device__ __forceinline__ float x16_add(float a, float b) {
  auto s = __builtin_amdgcn_permlane16_swap(__float_as_uint(a), __float_as_uint(b), false, false);
  return __uint_as_float(s[0]) + __uint_as_float(s[1]);
}
__device__ __forceinline__ float x32_add(float a, float b) {
  auto s = __builtin_amdgcn_permlane32_swap(__float_as_uint(a), __float_as_uint(b), false, false);
  return __uint_as_float(s[0]) + __uint_as_float(s[1]);
}
__device__ __forceinline__ float x16_max(float x) {
  auto s = __builtin_amdgcn_permlane16_swap(__float_as_uint(x), __float_as_uint(x), false, false);
  return fmaxf(__uint_as_float(s[0]), __uint_as_float(s[1]));
}
__device__ __forceinline__ float x32_max(float x) {
  auto s = __builtin_amdgcn_permlane32_swap(__float_as_uint(x), __float_as_uint(x), false, false);
  return fmaxf(__uint_as_float(s[0]), __uint_as_float(s[1]));
}
__device__ __forceinline__ float wave_sum(float x) { x = row_sum(x); x = x16_add(x, x); return x32_add(x, x); }
__device__ __forceinline__ float rdlane(float x, int l) { return __builtin_bit_cast(float, __builtin_amdgcn_readlane(__builtin_bit_cast(int, x), l)); }
__device__ __forceinline__ float rden_of(float var) {
  return __builtin_amdgcn_rcpf(rbf(__builtin_amdgcn_sqrtf(rbf(rbf(var) + 1e-5f))));
}
__device__ __forceinline__ f32x4 mfma(u32x4 a, u32x4 b, f32x4 c) {
  return __builtin_amdgcn_mfma_f32_16x16x32_bf16(__builtin_bit_cast(bf16x8, a), __builtin_bit_cast(bf16x8, b), c, 0, 0, 0);
}
__device__ __forceinline__ u32x4 read_tr(LAS char* a, LAS char* b) {
  const u32x2 ua = __builtin_bit_cast(u32x2, __builtin_amdgcn_ds_read_tr16_b64_v4i16((LAS s16x4*)a));
  const u32x2 ub = __builtin_bit_cast(u32x2, __builtin_amdgcn_ds_read_tr16_b64_v4i16((LAS s16x4*)b));
  return u32x4{ua[0], ua[1], ub[0], ub[1]};
}
#define SB() __builtin_amdgcn_sched_barrier(0)
#define PIN(x) do { asm volatile("" : "+v"(x) :: "memory"); __builtin_amdgcn_sched_barrier(0); } while (0)
#define PINM() do { asm volatile("" ::: "memory"); __builtin_amdgcn_sched_barrier(0); } while (0)
#define SINK(x) asm volatile("" :: "v"(x))
#define VMWAIT(n) do { __builtin_amdgcn_sched_barrier(0); asm volatile("s_waitcnt vmcnt(" #n ")" ::: "memory"); __builtin_amdgcn_sched_barrier(0); } while (0)
#define LDS_BARRIER() do { asm volatile("s_waitcnt lgkmcnt(0)" ::: "memory"); __builtin_amdgcn_s_barrier(); asm volatile("" ::: "memory"); } while (0)

constexpr float KS = 0.4686686930532063f;
constexpr float BETA = 2.f * LOG2E * 0.7978845608028654f / KS;
__device__ __forceinline__ f32x2 term(f32x2 h, f32x2 w1, f32x2 b1, f32x2 w2, f32x2 a) {
  const f32x2 v = __builtin_elementwise_fma(h, w1, b1);
  const f32x2 z = v * __builtin_elementwise_fma(v, v, f32x2{BETA, BETA});
  const f32x2 d = f32x2{__builtin_amdgcn_exp2f(-z[0]), __builtin_amdgcn_exp2f(-z[1])} + 1.f;
  return __builtin_elementwise_fma(v * w2, f32x2{__builtin_amdgcn_rcpf(d[0]), __builtin_amdgcn_rcpf(d[1])}, a);
}
template <int V> struct IC { static constexpr int value = V; };
template <int N, class F>
__device__ __forceinline__ void settle(u32x4 (&g)[N], unsigned tag, F load) {
#pragma unroll 1
  for (unsigned spin = 0; spin < SPIN_CAP; spin++) {
    unsigned y[N], any = 0;
#pragma unroll
    for (int s = 0; s < N; s++) { y[s] = __builtin_amdgcn_bitop3_b32(g[s][1], g[s][3], tag, 0x7e); any |= y[s]; }
    if (__builtin_amdgcn_ballot_w64(any != 0) == 0) break;
    asm volatile("" ::: "memory");
#pragma unroll
    for (int s = 0; s < N; s++)
      if (__builtin_amdgcn_ballot_w64(y[s] != 0) != 0) g[s] = load(s);
  }
}
__device__ __forceinline__ float fadd(float a, float b) { float r; asm("v_add_f32 %0, %1, %2" : "=v"(r) : "v"(a), "v"(b)); return r; }
__device__ __forceinline__ float sum8(const u32x4 (&g)[8], int c) {
  float t[4];
#pragma unroll
  for (int i = 0; i < 4; i++) t[i] = fadd(__uint_as_float(g[2 * i][c]), __uint_as_float(g[2 * i + 1][c]));
  return fadd(fadd(t[0], t[1]), fadd(t[2], t[3]));
}

__global__ void __launch_bounds__(512, 2) fused_kernel(Params p) {
  __shared__ __attribute__((aligned(16))) char lds_[L_END];
  LAS char* const lds = (LAS char*)lds_;
  const int tid = threadIdx.x, lane = tid & 63;
  const int wave = __builtin_amdgcn_readfirstlane(tid >> 6);
  const int bid = blockIdx.x;
  const int g = lane >> 4, n = lane & 15;
  const unsigned pidx = (bid & 0xee) | (bid & 1) << 4 | (bid >> 4 & 1), phi = pidx * T_SPAN >> 8;
  unsigned org = (unsigned)__builtin_amdgcn_s_memrealtime();
  char* pc; unsigned pclen;
  asm volatile("s_getpc_b64 %0\n.Lfused_first:\n\ts_mov_b32 %1, .Lfused_last-.Lfused_first" : "=s"(pc), "=s"(pclen));
  auto rc = __builtin_amdgcn_make_buffer_rsrc((void*)pc, 0, pclen, 0x00020000);
  const unsigned touch = __builtin_amdgcn_raw_buffer_load_b32(rc, tid * 64, 0, 0);
  PINM();

  const int ab = bid >> 7, kvh = (bid >> 3) & 15, sp = bid & 7;
  auto sprd = [&](unsigned v, int first, int nb) { unsigned r = 0; for (int i = 0; i < nb; i++) r |= ((v >> i) & 1u) << PB[first + i]; return r; };
  unsigned va[4];
#pragma unroll
  for (int t = 0; t < 4; t++) va[t] = sprd(g + 4 * t, 0, 4) + n * 16;
  const unsigned sbase = ((unsigned)(bid >> 3) << 23) + sprd(wave, 4, 3);
  auto turn_off = [&](int i) { return sbase + sprd(i, 10, 2) + sprd(sp, 12, 3); };
  unsigned so = turn_off(0);
#define SO(s) (so + sprd(s, 7, 3))
  u32x4 rb[NSL][4];
#define LOAD(s, rs) _Pragma("unroll") for (int t = 0; t < 4; t++) rb[s][t] = __builtin_amdgcn_raw_buffer_load_b128(rs, va[t], SO(s), AUX_NT | AUX_SC1)

  unsigned msk[4];
  auto mkmask = [&]() {
    int nn = n;
    asm volatile("" : "+v"(nn));
#pragma unroll
    for (int k = 0; k < 4; k++) msk[k] = (((nn & 7) >> 1) == k) ? ((nn & 1) ? 0xffff0000u : 0x0000ffffu) : 0u;
  };

  PINM();
  u32x3 xs[3], xm[3];
  u32x4 wb[18];
#pragma unroll
  for (int t = 0; t < 4; t++) PIN(va[t]);
  asm volatile("" : "+s"(so));
  PINM();
  const char* pk = p.K;
  asm volatile("" : "+s"(pk));
  auto rws = __builtin_amdgcn_make_buffer_rsrc((void*)p.ws, 0, WS_END, 0x00020000);
  auto rk = __builtin_amdgcn_make_buffer_rsrc((void*)pk, 0, 256u << 20, 0x00020000);
  auto rv = __builtin_amdgcn_make_buffer_rsrc((void*)p.V, 0, 256u << 20, 0x00020000);
  unsigned epoch = *(const __attribute__((address_space(4))) unsigned*)(p.ws + WS_EPOCH);
  if (wave == 0) {
    const int hq = bid >> 3, ks = bid & 7;
    auto rx = __builtin_amdgcn_make_buffer_rsrc((void*)p.x, 0, 2 * DM * 2, 0x00020000);
    auto rq = __builtin_amdgcn_make_buffer_rsrc((void*)p.wq, 0, DM * 4096 * 2, 0x00020000);
#pragma unroll
    for (int i = 0; i < 3; i++) xs[i] = __builtin_amdgcn_raw_buffer_load_b96(rx, lane * 36 + 12 * i, 0, 0);
#pragma unroll
    for (int i = 0; i < 3; i++) xm[i] = __builtin_amdgcn_raw_buffer_load_b96(rx, ((n >> 3) * 32 + 4 * ks + g) * 36 + 12 * i, 0, 0);
#pragma unroll
    for (int t = 0; t < 18; t++) {
      if (t == 10) { PINM(); asm volatile("s_barrier" ::: "memory"); PINM(); }
      wb[t] = __builtin_amdgcn_raw_buffer_load_b128(rq, g * 18 * 8192 + n * 16, (72 * ks + t) * 8192 + hq * 256, AUX_NT);
    }
    PINM();
    int one = 1;
    asm volatile("" : "+s"(one));
    if (__builtin_expect(one, 0)) {
    mkmask();
    float s1 = 0.f;
#pragma unroll
    for (int i = 0; i < 9; i++) { const unsigned u = xs[i / 3][i % 3]; s1 += lo2f(u) + hi2f(u); }
    s1 = row_sum(s1);
    const float mean = x16_add(s1, s1) * (1.f / DM);
    float s2 = 0.f;
#pragma unroll
    for (int i = 0; i < 9; i++) { const unsigned u = xs[i / 3][i % 3]; const float a = lo2f(u) - mean, c = hi2f(u) - mean; s2 += a * a + c * c; }
    s2 = row_sum(s2);
    const float rden = rden_of(x16_add(s2, s2) * (1.f / DM)), mb = rbf(mean);
    const float mb0 = rdlane(mb, 0), mb1 = rdlane(mb, 32), rd0 = rdlane(rden, 0), rd1 = rdlane(rden, 32);
    const float mb_ = (n < 8) ? mb0 : mb1, rd_ = (n < 8) ? rd0 : rd1;
    f32x4 qa = {0, 0, 0, 0};
#pragma unroll
    for (int i = 0; i < 9; i++) {
      const unsigned u = xm[i / 3][i % 3], d = cvtpk(lo2f(u) - mb_, hi2f(u) - mb_);
      const float y0 = lo2f(d) * rd_, y1 = hi2f(d) * rd_;
      const unsigned p0 = cvtpk(y0, y0), p1 = cvtpk(y1, y1);
      qa = mfma(u32x4{p0 & msk[0], p0 & msk[1], p0 & msk[2], p0 & msk[3]}, wb[2 * i], qa);
      qa = mfma(u32x4{p1 & msk[0], p1 & msk[1], p1 & msk[2], p1 & msk[3]}, wb[2 * i + 1], qa);
    }
    unsigned tag = epoch;
    asm volatile("" : "+s"(tag));
    tag += 1;
    __builtin_amdgcn_raw_buffer_store_b128(u32x4{__float_as_uint(qa[0]), tag, __float_as_uint(qa[1]), tag}, rws, lane * 16, WS_QG + bid * 2048, AUX_SC1);
    __builtin_amdgcn_raw_buffer_store_b128(u32x4{__float_as_uint(qa[2]), tag, __float_as_uint(qa[3]), tag}, rws, lane * 16, WS_QG + bid * 2048 + 1024, AUX_SC1);
    } else __builtin_amdgcn_s_waitcnt(0x0f70);
  }
  else asm volatile("s_barrier" ::: "memory");
  PINM();
#pragma unroll
  for (int t = 0; t < 4; t++) PIN(va[t]);
#pragma unroll
  for (int s = 0; s < NSL; s++) { LOAD(s, rk); PINM(); }
  asm volatile("" : "+s"(epoch));
  const unsigned tag = epoch + 1;
  const bool fin = bid >= 254;
  const int fb = bid & 1;
  const u32x4 w1v = *(const u32x4*)(p.w1 + 8 * tid), b1v = *(const u32x4*)(p.b1 + 8 * tid), w2v = *(const u32x4*)(p.w2 + 8 * tid);
  const unsigned w1t = p.w1[4096 + tid], b1t = p.b1[4096 + tid], w2t = p.w2[4096 + tid];
  const unsigned xh = *(const unsigned*)(p.x + fb * DM + 2 * min(tid, 287)), b2v = p.b2[0];
  PINM();
  u32x2 post = {0, 0};
  int ext = 0;
  auto pace = [&](int r, int s) {
    if (wave == PACER) {
      if ((s & 3) == 2 && r >= 10) {
        const bool ok = post[1] == tag, first = r == 10;
        const float o = (float)((int)((post[0] - org) << 12) >> 12);
        const float sum = wave_sum(fminf(fmaxf(ok ? o + (float)(post[0] >> 20) : first ? 0.f : 312.f, -400.f), 1000.f));
        const int cnt = first ? max(__builtin_popcountll(__builtin_amdgcn_ballot_w64(ok)), 1) : 64;
        const int late = __builtin_amdgcn_readfirstlane((int)(sum * __builtin_amdgcn_rcpf((float)cnt))) + 2 * ext - 10;
        int up = max(late, 0);
        if (first) {
          float m = ok ? fminf(o, 400.f) : -400.f;
          m = fmaxf(m, dpp<XOR1>(m)); m = fmaxf(m, dpp<XOR2>(m)); m = fmaxf(m, dpp<HMIR>(m)); m = fmaxf(m, dpp<MIR>(m));
          up = max(late - 15, __builtin_amdgcn_readfirstlane((int)x32_max(x16_max(m))) - 30);
        }
        org += up + ext;
        if (r >= 18) ext = max(min(ext + (late >> 3), 40), 0);
      }
      const unsigned tgt = org + T_A + (r * T_STEP >> 4) + phi;
      int d = (int)((unsigned)__builtin_amdgcn_s_memrealtime() - tgt);
      if ((s & 3) == 2) {
        if (pidx < 64 && lane == 0)
          __builtin_amdgcn_raw_buffer_store_b64(u32x2{(org & 0xfffffu) | (min(max(d, 0), 1000) * 5 >> 2) << 20, tag}, rws, 0, WS_M + (r >> 2) * 512 + pidx * 8, AUX_SC1);
        if (r >= 6) post = __builtin_amdgcn_raw_buffer_load_b64(rws, lane * 8, WS_M + ((r >> 2) - 1) * 512, AUX_SC1);
      }
#pragma unroll 1
      for (int k = 0; k < 4000 && d < 0; k++) d = (int)((unsigned)__builtin_amdgcn_s_memrealtime() - tgt);
    }
  };
  unsigned kw[4], kr[4];
#pragma unroll
  for (int t = 0; t < 4; t++) {
    kw[t] = L_WO + wave * 4096 + (4 * g + t) * 256 + ((n ^ (4 * g + t)) << 4);
    kr[t] = L_WO + wave * 4096 + n * 256 + (((4 * t + g) ^ n) << 4);
    PIN(kw[t]); PIN(kr[t]);
  }
  PINM();
  if (wave == 1 || wave == 2) {
    u32x4 pq[8];
    auto ldq = [&](int s) {
      return __builtin_amdgcn_raw_buffer_load_b128(rws, (lane >> 5) * 1024 + (32 * ab + (lane & 31)) * 16, WS_QG + ((2 * kvh + wave - 1) * 8 + s) * 2048, AUX_SC1);
    };
#pragma unroll
    for (int s = 0; s < 8; s++) pq[s] = ldq(s);
    settle<8>(pq, tag, ldq);
    const float qa = sum8(pq, 0), qc = sum8(pq, 2);
    *(LAS unsigned*)(lds + L_Q + (wave - 1) * 256 + (8 * n + 4 * (g & 1) + 2 * (lane >> 5)) * 2) = cvtpk(qa, qc);
  }
  SINK(touch);
  LDS_BARRIER();
  u32x4 qf[4];
#pragma unroll
  for (int s = 0; s < 4; s++) qf[s] = *(LAS u32x4*)(lds + L_Q + (n >> 3) * 256 + (32 * s + 8 * g) * 2);

  unsigned Z[NTURN][2];
  f32x2 W1[4], B1[4], W2[4];
  float W1t, B1t, W2t, ya[4];
  auto point1 = [&](int k, unsigned t) {
    const unsigned hb = t >= NTAB / 2 ? t - NTAB / 2 + TAB0 + 0x8000u : t + TAB0;
    const float h0 = lo2f(hb), h1 = lo2f(hb + 1);
    if (!(k & 1)) {
      const f32x2 tt = term(f32x2{h0, h1}, f32x2{W1t, W1t}, f32x2{B1t, B1t}, f32x2{W2t, W2t}, f32x2{0.f, 0.f});
      ya[k] = tt[0]; ya[k + 1] = tt[1];
      PIN(ya[k + 1]);
    }
    f32x2 a = {ya[k], 0.f};
#pragma unroll
    for (int j = 0; j < 4; j++) a = term(f32x2{h0, h0}, W1[j], B1[j], W2[j], a);
    ya[k] = a[0] + a[1];
    PIN(ya[k]);
  };
  constexpr int PT = 8, T0 = 1;
  auto kturn = [&](auto tab_, auto rs, int i) {
    constexpr bool tab = decltype(tab_)::value;
    unsigned z0 = 0, z1 = 0;
    auto points = [&](int k0, int k1) {
#pragma unroll
      for (int k = k0; k < k1; k++) {
        point1(k & 3, bid * NP + PT * (i - T0) + k);
        if ((k & 3) == 3) {
          const float r = row_sum(x16_add(x32_add(ya[0], ya[1]), x32_add(ya[2], ya[3])));
          if (n == 0) *(LAS float*)(lds + L_RED + (PT * (i - T0) + (k & ~3) + (g >> 1) + 2 * (g & 1)) * 32 + wave * 4) = r;
          PINM();
        }
      }
    };
#pragma unroll
    for (int s = 0; s < NSL; s++) {
      if (!(s & 1) && (tab || s)) { SB(); pace(8 * i + s, s); SB(); __builtin_amdgcn_s_barrier(); SB(); }
#pragma unroll
      for (int t = 0; t < 4; t++) *(LAS u32x4*)(lds + kw[t]) = rb[s][t];
      PINM(); LOAD(s, rs); PINM();
      f32x4 sc = {0, 0, 0, 0};
#pragma unroll
      for (int t = 0; t < 4; t++) sc = mfma(*(LAS u32x4*)(lds + kr[t]), qf[t], sc);
      const unsigned u0 = cvtpk(sc[0], sc[1]), u1 = cvtpk(sc[2], sc[3]);
      z0 = (n & 7) == s ? u0 : z0; z1 = (n & 7) == s ? u1 : z1;
      PIN(z0); PIN(z1);
      if (tab) { constexpr int c[9] = {0, 1, 2, 3, 5, 6, 7, 8, 8}; points(c[s], c[s + 1]); }
    }
#pragma unroll
    for (int j = 0; j < NTURN - 1; j++) { Z[j][0] = Z[j + 1][0]; Z[j][1] = Z[j + 1][1]; }
    Z[NTURN - 1][0] = z0; Z[NTURN - 1][1] = z1;
  };
  so = turn_off(1);
  kturn(IC<0>{}, rk, 0);
#pragma unroll
  for (int j = 0; j < 4; j++) {
    W1[j] = f32x2{lo2f(w1v[j]), hi2f(w1v[j])} * KS;
    B1[j] = f32x2{lo2f(b1v[j]), hi2f(b1v[j])} * KS;
    W2[j] = f32x2{lo2f(w2v[j]), hi2f(w2v[j])} * (1.f / KS);
    PIN(W1[j]); PIN(B1[j]); PIN(W2[j]);
  }
  W1t = lo2f(w1t) * KS; B1t = lo2f(b1t) * KS; W2t = lo2f(w2t) * (1.f / KS);
  PIN(W1t); PIN(B1t); PIN(W2t);
#pragma unroll 1
  for (int i = T0; i < NTURN; i++) {
    const bool last = i == NTURN - 1;
    so = turn_off(i + 1);
    kturn(IC<1>{}, __builtin_amdgcn_make_buffer_rsrc((void*)(last ? p.V : p.K), 0, 256u << 20, 0x00020000), i);
  }
  unsigned sel[2][4];
  {
    float z[4 * NTURN];
#pragma unroll
    for (int j = 0; j < 2 * NTURN; j++) {
      const unsigned u = Z[j >> 1][j & 1], w = cvtpk(lo2f(u) * SCALE, hi2f(u) * SCALE);
      z[2 * j] = lo2f(w); z[2 * j + 1] = hi2f(w);
    }
    float mx = z[0];
#pragma unroll
    for (int j = 1; j < 4 * NTURN; j++) mx = fmaxf(mx, z[j]);
    mx = fmaxf(mx, dpp<XOR1>(mx)); mx = fmaxf(mx, dpp<XOR2>(mx)); mx = fmaxf(mx, dpp<HMIR>(mx));
    mx = x32_max(x16_max(mx));
    if (lane == 0 || lane == 8) *(LAS float*)(lds + L_ML + (n >> 3) * 32 + wave * 4) = mx;
#pragma unroll
    for (int k = 0; k < 4; k++) {
      const bool on = ((n & 7) >> 1) == k;
      sel[0][k] = on ? ((n & 1) ? 0x01000c0cu : 0x0c0c0100u) : 0x0c0c0c0cu;
      sel[1][k] = on ? ((n & 1) ? 0x03020c0cu : 0x0c0c0302u) : 0x0c0c0c0cu;
    }
    LDS_BARRIER();
    if (wave == 1 && lane < NP) {
      const f32x4 a = *(LAS f32x4*)(lds + L_RED + lane * 32), c = *(LAS f32x4*)(lds + L_RED + lane * 32 + 16);
      __builtin_amdgcn_raw_buffer_store_b64(u32x2{__float_as_uint(((a[0] + a[1]) + (a[2] + a[3])) + ((c[0] + c[1]) + (c[2] + c[3]))), tag}, rws, lane * 8, WS_T + bid * NP * 8, AUX_SC1);
    }
    PINM();
    const f32x4 ma = *(LAS f32x4*)(lds + L_ML + (n >> 3) * 32), mb = *(LAS f32x4*)(lds + L_ML + (n >> 3) * 32 + 16);
    const float m2 = fmaxf(fmaxf(fmaxf(ma[0], ma[1]), fmaxf(ma[2], ma[3])), fmaxf(fmaxf(mb[0], mb[1]), fmaxf(mb[2], mb[3]))) * LOG2E;
    float l = 0.f;
#pragma unroll
    for (int j = 0; j < 2 * NTURN; j++) {
      const float ea = __builtin_amdgcn_exp2f(fmaf(z[2 * j], LOG2E, -m2)), eb = __builtin_amdgcn_exp2f(fmaf(z[2 * j + 1], LOG2E, -m2));
      l += ea + eb;
      Z[j >> 1][j & 1] = cvtpk(ea, eb);
      asm volatile("" : "+v"(Z[j >> 1][j & 1]));
    }
    l += dpp<XOR1>(l); l += dpp<XOR2>(l); l += dpp<HMIR>(l);
    l = x16_add(l, l); l = x32_add(l, l);
    if (lane == 0 || lane == 8) *(LAS float*)(lds + L_ML + 64 + (n >> 3) * 32 + wave * 4) = l;
    LDS_BARRIER();
    if (wave == 0) {
      const f32x4 la = *(LAS f32x4*)(lds + L_ML + 64 + (n >> 3) * 32), lb = *(LAS f32x4*)(lds + L_ML + 64 + (n >> 3) * 32 + 16);
      const float L = ((la[0] + la[1]) + (la[2] + la[3])) + ((lb[0] + lb[1]) + (lb[2] + lb[3]));
      if (lane == 0 || lane == 8)
        __builtin_amdgcn_raw_buffer_store_b128(u32x4{__float_as_uint(m2), tag, __float_as_uint(L), tag}, rws, (n >> 3) * 128, WS_ML + ((ab * 32 + 2 * kvh) * 8 + sp) * 16, AUX_SC1);
    }
  }
  const int rho = bid & 7, cg = bid >> 3;
  auto ldwo = [&](int row0, int cnt) {
    const u32x4 ro = {(unsigned)(uintptr_t)p.wo, (unsigned)((uintptr_t)p.wo >> 32) & 0xffffu, 4096 * DM * 2, 0x00020000};
    const unsigned vo = (lane >> 2) * 1152 + (((lane & 3) ^ ((lane >> 4) & 2)) << 4);
#pragma unroll
    for (int t = 0; t < cnt; t++)
      asm volatile("s_mov_b32 m0, %0\n\ts_nop 0\n\tbuffer_load_dwordx4 %1, %2, %3 offen nt lds"
                   :: "s"((unsigned)(uintptr_t)lds + L_WO + (row0 + 16 * t) * 64), "v"(vo), "s"(ro), "s"((512 * rho + row0 + 16 * t) * 1152 + 64 * cg) : "memory");
  };
  PINM();
  f32x4 acc = {0, 0, 0, 0};
  auto pv = [&](auto sc_, const unsigned (&P)[2], const u32x4 (&v)[4]) {
    constexpr int s = decltype(sc_)::value;
#pragma unroll
    for (int h = 0; h < 2; h++) {
      int b = __builtin_amdgcn_mov_dpp((int)P[h], 0x150 + s, 0xf, 0xf, true);
      b = __builtin_amdgcn_update_dpp(b, (int)P[h], 0x158 + s, 0xf, 0xc, false);
#pragma unroll
      for (int c = 0; c < 2; c++)
        acc = mfma(u32x4{__builtin_amdgcn_perm(b, b, sel[c][0]), __builtin_amdgcn_perm(b, b, sel[c][1]), __builtin_amdgcn_perm(b, b, sel[c][2]), __builtin_amdgcn_perm(b, b, sel[c][3])}, v[2 * h + c], acc);
    }
  };
  u32x4 gm[1];
  auto rml = __builtin_amdgcn_make_buffer_rsrc((void*)p.ws, 0, wave < 2 ? WS_END : 0, 0x00020000);
  auto ldm = [&](int) { return __builtin_amdgcn_raw_buffer_load_b128(rml, (lane & 7) * 16, WS_ML + (ab * 32 + 2 * kvh + wave) * 128, AUX_SC1); };
#define FOR8(X) X(0) X(1) X(2) X(3) X(4) X(5) X(6) X(7)
#pragma unroll 1
  for (int i = 0; i < NTURN - 1; i++) {
    so = turn_off(i + 1);
    gm[0] = ldm(0);
    if (fin && i == NTURN - 2) {
      const u32x4 rt = {(unsigned)(uintptr_t)p.ws, (unsigned)((uintptr_t)p.ws >> 32) & 0xffffu, WS_END, 0x00020000};
#pragma unroll
      for (int t = 0; t < NP / 4; t++)
        asm volatile("s_mov_b32 m0, %0\n\ts_nop 0\n\tbuffer_load_dwordx4 %1, %2, %3 offen sc1 lds"
                     :: "s"((unsigned)(uintptr_t)lds + L_TAB + (wave * (NP / 4) + t) * 1024), "v"(lane * 16), "s"(rt), "s"(WS_T + (wave * (NP / 4) + t) * 1024) : "memory");
    }
    PINM();
#define VSTEP(s) if (!(s & 1)) { SB(); pace(32 + 8 * i + s, s); SB(); __builtin_amdgcn_s_barrier(); SB(); } pv(IC<s>{}, Z[0], rb[s]); PIN(acc); LOAD(s, rv); SB();
    FOR8(VSTEP)
#pragma unroll
    for (int j = 0; j < NTURN - 1; j++) { Z[j][0] = Z[j + 1][0]; Z[j][1] = Z[j + 1][1]; }
  }
  const unsigned hrow = (wave >> 2) * 32 + 4 * rho + (wave & 3);
  auto ld1 = [&](int s) { return __builtin_amdgcn_raw_buffer_load_b128(rws, lane * 16, WS_P1 + hrow * 8192 + s * 1024, AUX_SC1); };
  u32x4 go[8], bfr[16];
  float wgt = 0.f;
  {
    __builtin_amdgcn_s_barrier();
    if (wave < 2) {
      if (__builtin_amdgcn_ballot_w64(__builtin_amdgcn_bitop3_b32(gm[0][1], gm[0][3], tag, 0x7e) != 0) != 0) { settle<1>(gm, tag, ldm); __builtin_amdgcn_s_waitcnt(0x0f70); }
      const float ms = __uint_as_float(gm[0][0]);
      float M = fmaxf(ms, dpp<XOR1>(ms)); M = fmaxf(M, dpp<XOR2>(M)); M = fmaxf(M, dpp<HMIR>(M));
      const float e = __builtin_amdgcn_exp2f(ms - M);
      float L = e * __uint_as_float(gm[0][2]); L += dpp<XOR1>(L); L += dpp<XOR2>(L); L += dpp<HMIR>(L);
      wgt = rdlane(e * __builtin_amdgcn_rcpf(L), sp);
    }
    PINM();
#define VLAST(s) pv(IC<s>{}, Z[0], rb[s]); SB();
    FOR8(VLAST)
    PIN(acc);
    PINM();
    if (wave < 4 && cg < 18) ldwo(128 * wave, 8);
    SB();
  }
  *(LAS f32x4*)(lds + L_O + wave * 1024 + (g >> 1) * 512 + (8 * n + 4 * (g & 1)) * 4) = acc;
  LDS_BARRIER();
  if (wave < 2) {
    f32x2 o[8];
#pragma unroll
    for (int w = 0; w < 8; w++) o[w] = *(LAS f32x2*)(lds + L_O + w * 1024 + wave * 512 + lane * 8);
    const f32x2 t = (((o[0] + o[1]) + (o[2] + o[3])) + ((o[4] + o[5]) + (o[6] + o[7]))) * wgt;
    __builtin_amdgcn_raw_buffer_store_b128(u32x4{__float_as_uint(t[0]), tag, __float_as_uint(t[1]), tag}, rws, lane * 16, WS_P1 + ((ab * 32 + 2 * kvh + wave) * 8 + sp) * 1024, AUX_SC1);
  }
  if (cg < 18) {
    unsigned olw = L_OL + (wave >> 2) * 1088 + (wave & 3) * 256 + lane * 4, olr = L_OL + (n & 1) * 1088 + 16 * g, p2o = (g * 8 * DM + n) * 8;
#pragma unroll
    for (int s = 0; s < 8; s++) go[s] = ld1(s);
    PIN(olw); PIN(olr); PIN(p2o);
    VMWAIT(8);
    __builtin_amdgcn_s_barrier();
    PINM();
    if (wave < 2) {
#pragma unroll
      for (int s = 0; s < 16; s++) {
        const int a = L_WO + (32 * s + 8 * g + (n >> 2)) * 64 + ((wave ^ (g & 1)) << 5) + (n & 3) * 8;
        bfr[s] = read_tr(lds + a, lds + a + 256);
      }
    }
    settle<8>(go, tag, ld1);
    const float o0 = sum8(go, 0), o1 = sum8(go, 2);
    *(LAS unsigned*)(lds + olw) = cvtpk(o0, o1);
    LDS_BARRIER();
    if (wave < 2) {
      u32x4 af[16];
#pragma unroll
      for (int s = 0; s < 16; s++) af[s] = *(LAS u32x4*)(lds + olr + 64 * s);
      SB();
      f32x4 d[4] = {};
#pragma unroll
      for (int s = 0; s < 16; s++) d[s & 3] = mfma(af[s], bfr[s], d[s & 3]);
      const float r = g ? (d[0][1] + d[1][1]) + (d[2][1] + d[3][1]) : (d[0][0] + d[1][0]) + (d[2][0] + d[3][0]);
      if (g < 2) __builtin_amdgcn_raw_buffer_store_b64(u32x2{__float_as_uint(r), tag}, rws, p2o, WS_P2 + (rho * DM + 32 * cg + 16 * wave) * 8, AUX_SC1);
    }
  }
  PINM();
  if (fin) {
    unsigned hh = 0;
    if (wave < 5) {
      u32x4 gp[8];
      auto ld2 = [&](int s) { return __builtin_amdgcn_raw_buffer_load_b128(rws, min(tid, 287) * 16, WS_P2 + (fb * 8 + s) * DM * 8, AUX_SC1); };
#pragma unroll
      for (int s = 0; s < 8; s++) gp[s] = ld2(s);
      settle<8>(gp, tag, ld2);
      const unsigned at = cvtpk(sum8(gp, 0), sum8(gp, 2));
      hh = cvtpk(lo2f(xh) + lo2f(at), hi2f(xh) + hi2f(at));
      if (tid < 288) *(LAS f32x2*)(lds + L_HS + tid * 8) = f32x2{lo2f(hh), hi2f(hh)};
    }
    LDS_BARRIER();
    if (wave == 7) {
      if (fb == 0 && lane == 0) __builtin_amdgcn_raw_buffer_store_b32(tag, rws, 0, WS_EPOCH, AUX_SC1);
    } else if (wave < 5) {
      float v[9];
#pragma unroll
      for (int i = 0; i < 9; i++) v[i] = *(LAS float*)(lds + L_HS + (lane + 64 * i) * 4);
      const float mean = wave_sum(((v[0] + v[1]) + (v[2] + v[3])) + ((v[4] + v[5]) + (v[6] + v[7])) + v[8]) * (1.f / DM);
      float s2a = 0.f, s2b = 0.f;
#pragma unroll
      for (int i = 0; i < 9; i++) { const float a = v[i] - mean; if (i & 1) s2b = fmaf(a, a, s2b); else s2a = fmaf(a, a, s2a); }
      const float var = wave_sum(s2a + s2b) * (1.f / DM);
      const float rden = rden_of(var), mb = rbf(mean);
      const float mb2 = rbf((mean - mb) * rden), rden2 = rden_of(var * rden * rden);
      unsigned u = cvtpk(lo2f(hh) - mb, hi2f(hh) - mb);
      u = cvtpk(lo2f(u) * rden, hi2f(u) * rden);
      u = cvtpk(lo2f(u) - mb2, hi2f(u) - mb2);
      u = cvtpk(lo2f(u) * rden2, hi2f(u) * rden2);
      const unsigned m0 = (u & 0x7fffu) - TAB0, m1 = ((u >> 16) & 0x7fffu) - TAB0;
      const bool in0 = m0 < NTAB / 2, in1 = m1 < NTAB / 2;
      const unsigned t0 = in0 ? m0 + (u >> 15 & 1) * (NTAB / 2) : 0, t1 = in1 ? m1 + (u >> 31) * (NTAB / 2) : 0;
      u32x2 f0 = *(LAS u32x2*)(lds + L_TAB + t0 * 8), f1 = *(LAS u32x2*)(lds + L_TAB + t1 * 8);
      if (__builtin_amdgcn_ballot_w64(!in0 || !in1 || f0[1] != tag || f1[1] != tag) != 0) {
#pragma unroll 1
        for (unsigned spin = 0; spin < SPIN_CAP && __builtin_amdgcn_ballot_w64(f0[1] != tag || f1[1] != tag) != 0; spin++) {
          f0 = __builtin_amdgcn_raw_buffer_load_b64(rws, t0 * 8, WS_T, AUX_SC1);
          f1 = __builtin_amdgcn_raw_buffer_load_b64(rws, t1 * 8, WS_T, AUX_SC1);
        }
#pragma unroll 1
        for (int c = 0; c < 2; c++) {
          unsigned long long todo = __builtin_amdgcn_ballot_w64(c ? !in1 : !in0);
#pragma unroll 1
          while (todo) {
            const int l = __builtin_ctzll(todo);
            todo &= todo - 1;
            const unsigned ul = __builtin_amdgcn_readlane(u, l);
            const float h = c ? hi2f(ul) : lo2f(ul);
            f32x2 a = {0.f, 0.f};
#pragma unroll 1
            for (int j = 0; j < FF / 512; j++) {
              const u32x4 a1 = *(const u32x4*)(p.w1 + 512 * j + 8 * lane), a2 = *(const u32x4*)(p.b1 + 512 * j + 8 * lane), a3 = *(const u32x4*)(p.w2 + 512 * j + 8 * lane);
#pragma unroll
              for (int k = 0; k < 4; k++)
                a = term(f32x2{h, h}, f32x2{lo2f(a1[k]), hi2f(a1[k])} * KS, f32x2{lo2f(a2[k]), hi2f(a2[k])} * KS, f32x2{lo2f(a3[k]), hi2f(a3[k])} * (1.f / KS), a);
            }
            const float r = wave_sum(a[0] + a[1]);
            if (lane == l) { if (c) f1[0] = __float_as_uint(r); else f0[0] = __float_as_uint(r); }
          }
        }
      }
      const float b2 = lo2f(b2v);
      unsigned y = cvtpk(__uint_as_float(f0[0]), __uint_as_float(f1[0]));
      y = cvtpk(lo2f(y) + b2, hi2f(y) + b2);
      auto rout = __builtin_amdgcn_make_buffer_rsrc((void*)p.out, 0, 2 * DM * 2, 0x00020000);
      if (tid < 288) __builtin_amdgcn_raw_buffer_store_b32(cvtpk(lo2f(hh) + lo2f(y), hi2f(hh) + hi2f(y)), rout, tid * 4, fb * DM * 2, AUX_SC1);
    }
  }
  asm volatile(".Lfused_last:");
}

extern "C" void kernel_launch(void* const* d_in, const int* in_sizes, int n_in, void* d_out, int out_size, void* d_ws, size_t ws_size, hipStream_t stream) {
  Params p;
  p.x = (const u16*)d_in[0]; p.K = (const char*)d_in[1]; p.V = (const char*)d_in[2];
  p.wq = (const char*)d_in[3]; p.wo = (const char*)d_in[4];
  p.w1 = (const u16*)d_in[6]; p.b1 = (const u16*)d_in[7]; p.w2 = (const u16*)d_in[8]; p.b2 = (const u16*)d_in[9];
  p.out = (u16*)d_out; p.ws = (char*)d_ws;
  fused_kernel<<<256, 512, 0, stream>>>(p);
}
```
